```python
import torch

def _softmax(x, axis=-1):
    return torch.softmax(x, dim=axis)
def _gelu(x):
    return torch.nn.functional.gelu(x, approximate="tanh")
def _silu(x):
    return torch.nn.functional.silu(x)
def _sigmoid(x):
    return torch.sigmoid(x)
def _layernorm(x, axis=-1, eps=1e-5):
    m = x.mean(dim=axis, keepdim=True)
    v = x.var(dim=axis, keepdim=True, unbiased=False)
    return (x - m) / torch.sqrt(v + eps)
def _scatter_add(tgt, idx, src, axis=0):
    return tgt.clone().index_add_(axis, idx, src)
def _segment_sum(src, seg, n):
    out = torch.zeros((n,) + src.shape[1:], dtype=src.dtype)
    return out.index_add_(0, seg, src)
def _pad(x, pw):
    flat = tuple(v for pair in reversed(pw) for v in pair)
    return torch.nn.functional.pad(x, flat)

kernel_name = 'standalone__glu_gated_geglu__decode'

def setup_inputs():

    _w = (lambda a: (lambda t: t.to(torch.bfloat16) if t.is_floating_point() else t)(torch.as_tensor(a)))
    B, S, Dm, Df = 1, 1, 1024, 3584
    return {
        "x": _w((lambda *s: torch.randn(*s, dtype=torch.bfloat16) * 0.05)(B, S, Dm)),
        "w_gate": _w((lambda *s: torch.randn(*s, dtype=torch.bfloat16) * 0.05)(Dm, Df) * (Dm ** -0.5)),
        "w_up": _w((lambda *s: torch.randn(*s, dtype=torch.bfloat16) * 0.05)(Dm, Df) * (Dm ** -0.5)),
        "w_down": _w((lambda *s: torch.randn(*s, dtype=torch.bfloat16) * 0.05)(Df, Dm) * (Df ** -0.5)),
    }

def reference(x, w_gate, w_up, w_down):
    g = torch.einsum("bsd,df->bsf", x, w_gate)
    u = torch.einsum("bsd,df->bsf", x, w_up)
    h = _gelu(g) * u
    return torch.einsum("bsf,fd->bsd", h, w_down)
```

```cpp
#include <hip/hip_runtime.h>
#include <hip/hip_bf16.h>

#define GAS __attribute__((address_space(1)))
#define LAS __attribute__((address_space(3)))
typedef unsigned v4u __attribute__((ext_vector_type(4)));
typedef unsigned v2u __attribute__((ext_vector_type(2)));
typedef float v4f __attribute__((ext_vector_type(4)));
typedef __bf16 bf16x2 __attribute__((ext_vector_type(2)));

constexpr int DM = 1024, DF = 3584;
constexpr int NPROD = 224;
constexpr int GRAN_OFF = 4096;
constexpr unsigned kSpinCap = 1u << 20;

__device__ __forceinline__ float dot2(unsigned a, unsigned b, float c) {
    return __builtin_amdgcn_fdot2_f32_bf16(__builtin_bit_cast(bf16x2, a), __builtin_bit_cast(bf16x2, b), c, false);
}
__device__ __forceinline__ unsigned plo(unsigned r0, unsigned r1) { return __builtin_amdgcn_perm(r1, r0, 0x05040100u); }
__device__ __forceinline__ unsigned phi(unsigned r0, unsigned r1) { return __builtin_amdgcn_perm(r1, r0, 0x07060302u); }
__device__ __forceinline__ float bf2f(unsigned b) { return __builtin_bit_cast(float, b << 16); }
__device__ __forceinline__ unsigned f2bf(float f) {
    __bf16 h = (__bf16)f; return (unsigned)__builtin_bit_cast(unsigned short, h);
}
template <int CTRL> __device__ __forceinline__ float dpp(float v) {
    return __builtin_bit_cast(float, __builtin_amdgcn_update_dpp(0, __builtin_bit_cast(int, v), CTRL, 0xf, 0xf, true));
}

__global__ void __launch_bounds__(256, 1)
glu_kernel(const unsigned short* __restrict__ x_, const unsigned short* __restrict__ wg_, const unsigned short* __restrict__ wu_,
           const unsigned short* __restrict__ wd_, unsigned short* __restrict__ out_, unsigned* __restrict__ ws_) {
    __shared__ __attribute__((aligned(16))) float red[32 * 4];
    __shared__ __attribute__((aligned(16))) float red2[4 * 8];
    const int b = blockIdx.x, tid = threadIdx.x, lane = tid & 63;
    const int w = __builtin_amdgcn_readfirstlane(tid >> 6);
    const int s = (b & 7) * 28 + (b >> 3);
    const int j = lane >> 1, c = lane & 1;

    const GAS unsigned* xw = (const GAS unsigned*)x_;
    unsigned xp[4];
#pragma unroll
    for (int t = 0; t < 4; ++t) xp[t] = xw[128 * w + 32 * t + j];

    const GAS unsigned char* gp = (const GAS unsigned char*)wg_ + (size_t)(256 * w + 2 * j) * (DF * 2) + s * 32 + c * 16;
    const GAS unsigned char* up = (const GAS unsigned char*)wu_ + (size_t)(256 * w + 2 * j) * (DF * 2) + s * 32 + c * 16;
    v4u g0[4], g1[4], u0[4], u1[4];
#pragma unroll
    for (int t = 0; t < 4; ++t) {
        g0[t] = __builtin_nontemporal_load((const GAS v4u*)(gp + (size_t)(64 * t) * (DF * 2)));
        g1[t] = __builtin_nontemporal_load((const GAS v4u*)(gp + (size_t)(64 * t + 1) * (DF * 2)));
        u0[t] = __builtin_nontemporal_load((const GAS v4u*)(up + (size_t)(64 * t) * (DF * 2)));
        u1[t] = __builtin_nontemporal_load((const GAS v4u*)(up + (size_t)(64 * t + 1) * (DF * 2)));
    }
    const GAS unsigned char* dp = (const GAS unsigned char*)wd_ + (size_t)(16 * s) * (DM * 2) + tid * 8;
    v2u d[16];
#pragma unroll
    for (int r = 0; r < 16; ++r) d[r] = __builtin_nontemporal_load((const GAS v2u*)(dp + r * (DM * 2)));
    const unsigned T = *(const GAS unsigned*)ws_ + 1u;
    __builtin_amdgcn_sched_barrier(0);

    float a[16];
#pragma unroll
    for (int i = 0; i < 16; ++i) a[i] = 0.f;
#pragma unroll
    for (int t = 0; t < 4; ++t) {
#pragma unroll
        for (int k = 0; k < 4; ++k) {
            a[2 * k]         = dot2(plo(g0[t][k], g1[t][k]), xp[t], a[2 * k]);
            a[2 * k + 1]     = dot2(phi(g0[t][k], g1[t][k]), xp[t], a[2 * k + 1]);
            a[8 + 2 * k]     = dot2(plo(u0[t][k], u1[t][k]), xp[t], a[8 + 2 * k]);
            a[8 + 2 * k + 1] = dot2(phi(u0[t][k], u1[t][k]), xp[t], a[8 + 2 * k + 1]);
        }
    }
#pragma unroll
    for (int i = 0; i < 16; ++i) {
        float v = a[i];
        v += __shfl_xor(v, 2); v += __shfl_xor(v, 4); v += __shfl_xor(v, 8); v += __shfl_xor(v, 16); v += __shfl_xor(v, 32);
        a[i] = v;
    }
    if (lane < 2) {
#pragma unroll
        for (int i = 0; i < 8; ++i) {
            red[(8 * c + i) * 4 + w] = a[i];
            red[(16 + 8 * c + i) * 4 + w] = a[8 + i];
        }
    }
    __syncthreads();
    unsigned hp[8];
    {
        const int col = lane & 15;
        const v4f gv = *(const v4f*)&red[col * 4];
        const v4f uv = *(const v4f*)&red[(16 + col) * 4];
        const float g = bf2f(f2bf((gv[0] + gv[1]) + (gv[2] + gv[3])));
        const float u = bf2f(f2bf((uv[0] + uv[1]) + (uv[2] + uv[3])));
        const float y2 = 1.5957691216057308f * (g + 0.044715f * g * g * g);
        const float ge = g / (1.0f + __expf(-y2));
        const float act = bf2f(f2bf(ge));
        const unsigned h = f2bf(act * u);
        const unsigned hn = (unsigned)__builtin_amdgcn_update_dpp(0, (int)h, 0xB1  , 0xf, 0xf, true);
        const unsigned pr = h | (hn << 16);
#pragma unroll
        for (int k = 0; k < 8; ++k) hp[k] = __builtin_amdgcn_readlane(pr, 2 * k);
    }
    float o[4] = {0.f, 0.f, 0.f, 0.f};
#pragma unroll
    for (int k = 0; k < 8; ++k) {
#pragma unroll
        for (int m = 0; m < 2; ++m) {
            o[2 * m]     = dot2(plo(d[2 * k][m], d[2 * k + 1][m]), hp[k], o[2 * m]);
            o[2 * m + 1] = dot2(phi(d[2 * k][m], d[2 * k + 1][m]), hp[k], o[2 * m + 1]);
        }
    }
    const __amdgpu_buffer_rsrc_t rs = __builtin_amdgcn_make_buffer_rsrc((void*)((unsigned char*)ws_ + GRAN_OFF), 0, NPROD * 8192, 0x00020000);
    {
        v4u p0 = {__float_as_uint(o[0]), T, __float_as_uint(o[1]), T};
        v4u p1 = {__float_as_uint(o[2]), T, __float_as_uint(o[3]), T};
        __builtin_amdgcn_raw_buffer_store_b128(p0, rs, s * 8192 + tid * 32, 0, 16);
        __builtin_amdgcn_raw_buffer_store_b128(p1, rs, s * 8192 + tid * 32 + 16, 0, 16);
    }
    if (b < 128) {
        const int cg = (b & 7) * 16 + (b >> 3);
        const int pr = lane & 3, ps = lane >> 2;
        float s0 = 0.f, s1 = 0.f;
        unsigned need = 0;
#pragma unroll
        for (int k = 0; k < 4; ++k) if (16 * k + ps < 56) need |= 1u << k;
        v4u val[4];
        for (unsigned spins = 0;;) {
#pragma unroll
            for (int k = 0; k < 4; ++k) {
                const int p = 56 * w + 16 * k + ps;
                if (need & (1u << k)) val[k] = __builtin_amdgcn_raw_buffer_load_b128(rs, p * 8192 + cg * 64 + pr * 16, 0, 16);
            }
#pragma unroll
            for (int k = 0; k < 4; ++k) {
                if ((need & (1u << k)) && val[k][1] == T && val[k][3] == T) {
                    s0 += __uint_as_float(val[k][0]); s1 += __uint_as_float(val[k][2]);
                    need &= ~(1u << k);
                }
            }
            if (__all(need == 0)) break;
            if (++spins > kSpinCap) break;
            __builtin_amdgcn_s_sleep(1);
        }
#pragma unroll
        for (int m = 4; m <= 32; m <<= 1) { s0 += __shfl_xor(s0, m); s1 += __shfl_xor(s1, m); }
        if (lane < 4) { red2[w * 8 + 2 * lane] = s0; red2[w * 8 + 2 * lane + 1] = s1; }
        __syncthreads();
        if (tid < 4) {
            const float r0 = (red2[2 * tid] + red2[8 + 2 * tid]) + (red2[16 + 2 * tid] + red2[24 + 2 * tid]);
            const float r1 = (red2[2 * tid + 1] + red2[8 + 2 * tid + 1]) + (red2[16 + 2 * tid + 1] + red2[24 + 2 * tid + 1]);
            ((GAS unsigned*)out_)[cg * 4 + tid] = f2bf(r0) | (f2bf(r1) << 16);
        }
        if (b == 0 && tid == 0) *(GAS unsigned*)ws_ = T;
    }
}

extern "C" void kernel_launch(void* const* d_in, const int* in_sizes, int n_in, void* d_out, int out_size, void* d_ws, size_t ws_size,
                              hipStream_t stream) {
    glu_kernel<<<NPROD, 256, 0, stream>>>((const unsigned short*)d_in[0], (const unsigned short*)d_in[1], (const unsigned short*)d_in[2],
                                          (const unsigned short*)d_in[3], (unsigned short*)d_out, (unsigned*)d_ws);
}
```

```cpp
#include <hip/hip_runtime.h>
#include <hip/hip_bf16.h>

#define GAS __attribute__((address_space(1)))
typedef unsigned v4u __attribute__((ext_vector_type(4)));
typedef unsigned v2u __attribute__((ext_vector_type(2)));
typedef float v4f __attribute__((ext_vector_type(4)));
typedef __bf16 bf16x2 __attribute__((ext_vector_type(2)));

#define DOWN_POS 0
#define AUXW 2
#define SAME_XCD 1

constexpr int DM = 1024, DF = 3584;
constexpr int NWG = 224;
constexpr int P1_OFF = 4096;
constexpr int Q_OFF = P1_OFF + 56 * 4 * 1024;
constexpr int WS_USED = Q_OFF + 56 * 8192;
constexpr unsigned kSpinCap = 1u << 18;
#define STAMP(i) do {} while (0)

__device__ __forceinline__ float dot2(unsigned a, unsigned b, float c) {
    return __builtin_amdgcn_fdot2_f32_bf16(__builtin_bit_cast(bf16x2, a), __builtin_bit_cast(bf16x2, b), c, false);
}
__device__ __forceinline__ unsigned plo(unsigned r0, unsigned r1) { return __builtin_amdgcn_perm(r1, r0, 0x05040100u); }
__device__ __forceinline__ unsigned phi(unsigned r0, unsigned r1) { return __builtin_amdgcn_perm(r1, r0, 0x07060302u); }
__device__ __forceinline__ float bf2f(unsigned b) { return __uint_as_float(b << 16); }
__device__ __forceinline__ unsigned f2bf(float f) { __bf16 h = (__bf16)f; return (unsigned)__builtin_bit_cast(unsigned short, h); }
template <int CTRL> __device__ __forceinline__ unsigned dppu(unsigned v) { return (unsigned)__builtin_amdgcn_update_dpp(0, (int)v, CTRL, 0xf, 0xf, true); }
template <int CTRL> __device__ __forceinline__ float dppf(float v) { return __uint_as_float(dppu<CTRL>(__float_as_uint(v))); }
__device__ __forceinline__ float tr32(float a, float b) { auto r = __builtin_amdgcn_permlane32_swap(__float_as_uint(a), __float_as_uint(b), false, false); return __uint_as_float(r[0]) + __uint_as_float(r[1]); }
__device__ __forceinline__ float tr16(float a, float b) { auto r = __builtin_amdgcn_permlane16_swap(__float_as_uint(a), __float_as_uint(b), false, false); return __uint_as_float(r[0]) + __uint_as_float(r[1]); }
__device__ __forceinline__ float tr8(float a, float b, bool bit3) { const float a2 = a + dppf<0x128>(a), b2 = b + dppf<0x128>(b); return bit3 ? b2 : a2; }

__global__ void __launch_bounds__(256, 1)
glu_kernel(const unsigned short* __restrict__ x_, const unsigned short* __restrict__ wg_, const unsigned short* __restrict__ wu_,
           const unsigned short* __restrict__ wd_, unsigned short* __restrict__ out_, unsigned* __restrict__ ws_) {
    __shared__ __attribute__((aligned(16))) float red[4 * 128];
    const int b = blockIdx.x, tid = threadIdx.x, lane = tid & 63;
    const int w = __builtin_amdgcn_readfirstlane(tid >> 6);
    const int gi = (b & 7) * 7 + (b >> 5), gj = (b >> 3) & 3;
    const int r = lane >> 3, cg = lane & 7;
    STAMP(0);
    const __amdgpu_buffer_rsrc_t rg = __builtin_amdgcn_make_buffer_rsrc((void*)wg_, 0, DM * DF * 2, 0x00020000);
    const __amdgpu_buffer_rsrc_t ru = __builtin_amdgcn_make_buffer_rsrc((void*)wu_, 0, DM * DF * 2, 0x00020000);
    const __amdgpu_buffer_rsrc_t rd = __builtin_amdgcn_make_buffer_rsrc((void*)wd_, 0, DM * DF * 2, 0x00020000);
    const __amdgpu_buffer_rsrc_t rs = __builtin_amdgcn_make_buffer_rsrc((void*)ws_, 0, WS_USED, 0x00020000);

    const v4u xp = *(const GAS v4u*)((const GAS unsigned char*)x_ + (256 * gj + 64 * w + 8 * r) * 2);
    const int b2 = (lane >> 2) & 1;
    const int dvA = (8 * r + 4 * b2) * (DM * 2) + cg * 16, dvB = (8 * r + 4 * (1 - b2)) * (DM * 2) + cg * 16;
    const int dso = (64 * gi) * (DM * 2) + 512 * gj + 128 * w;
    v4u d0[4], d1[4];
#define LOAD_DOWN() do { \
        d0[0] = __builtin_amdgcn_raw_buffer_load_b128(rd, dvA, dso, AUXW);            d1[0] = __builtin_amdgcn_raw_buffer_load_b128(rd, dvA + 2048, dso, AUXW); \
        d0[1] = __builtin_amdgcn_raw_buffer_load_b128(rd, dvA, dso + 4096, AUXW);     d1[1] = __builtin_amdgcn_raw_buffer_load_b128(rd, dvA + 2048, dso + 4096, AUXW); \
        d0[2] = __builtin_amdgcn_raw_buffer_load_b128(rd, dvB, dso, AUXW);            d1[2] = __builtin_amdgcn_raw_buffer_load_b128(rd, dvB + 2048, dso, AUXW); \
        d0[3] = __builtin_amdgcn_raw_buffer_load_b128(rd, dvB, dso + 4096, AUXW);     d1[3] = __builtin_amdgcn_raw_buffer_load_b128(rd, dvB + 2048, dso + 4096, AUXW); \
        __builtin_amdgcn_sched_barrier(0); } while (0)
    const int gv = (8 * r) * (DF * 2) + cg * 16;
    const int gso = (256 * gj + 64 * w) * (DF * 2) + 128 * gi;
    v4u g0[4], g1[4], u0[4], u1[4];
#pragma unroll
    for (int t = 0; t < 4; ++t) {
        g0[t] = __builtin_amdgcn_raw_buffer_load_b128(rg, gv, gso + (2 * t) * (DF * 2), AUXW);
        g1[t] = __builtin_amdgcn_raw_buffer_load_b128(rg, gv, gso + (2 * t + 1) * (DF * 2), AUXW);
        u0[t] = __builtin_amdgcn_raw_buffer_load_b128(ru, gv, gso + (2 * t) * (DF * 2), AUXW);
        u1[t] = __builtin_amdgcn_raw_buffer_load_b128(ru, gv, gso + (2 * t + 1) * (DF * 2), AUXW);
    }
    __builtin_amdgcn_sched_barrier(0);
    LOAD_DOWN();
    const unsigned T = *(const GAS unsigned*)ws_ + 1u;
    STAMP(1);

    float a[16];
#pragma unroll
    for (int i = 0; i < 16; ++i) a[i] = 0.f;
#pragma unroll
    for (int t = 0; t < 4; ++t) {
        const unsigned xx = xp[t];
#pragma unroll
        for (int k = 0; k < 4; ++k) {
            a[2 * k]         = dot2(plo(g0[t][k], g1[t][k]), xx, a[2 * k]);
            a[2 * k + 1]     = dot2(phi(g0[t][k], g1[t][k]), xx, a[2 * k + 1]);
            a[8 + 2 * k]     = dot2(plo(u0[t][k], u1[t][k]), xx, a[8 + 2 * k]);
            a[8 + 2 * k + 1] = dot2(phi(u0[t][k], u1[t][k]), xx, a[8 + 2 * k + 1]);
        }
    }
    STAMP(2);
    const bool bit3 = (lane & 8) != 0;
    {
        float y[8], z[4];
#pragma unroll
        for (int k = 0; k < 8; ++k) y[k] = tr32(a[k], a[k + 8]);
#pragma unroll
        for (int k = 0; k < 4; ++k) z[k] = tr16(y[k], y[k + 4]);
        float2 q; q.x = tr8(z[0], z[2], bit3); q.y = tr8(z[1], z[3], bit3);
        const int m = lane >> 5, col0 = 8 * cg + 4 * ((lane >> 4) & 1) + 2 * ((lane >> 3) & 1);
        *(float2*)&red[w * 128 + m * 64 + col0] = q;
    }
    __syncthreads();
    float g = (red[lane] + red[128 + lane]) + (red[256 + lane] + red[384 + lane]);
    float u = (red[64 + lane] + red[128 + 64 + lane]) + (red[256 + 64 + lane] + red[384 + 64 + lane]);
    if (w == 0) {
        const v4u p = {__float_as_uint(g), T, __float_as_uint(u), T};
        __builtin_amdgcn_raw_buffer_store_b128(p, rs, P1_OFF + (gi * 4 + gj) * 1024 + lane * 16, 0, 16);
    }
    STAMP(3);
    {
        v4u pv[4];
        for (unsigned spins = 0;;) {
            asm volatile("" ::: "memory");
            bool ok = true;
#pragma unroll
            for (int m = 0; m < 4; ++m) pv[m] = __builtin_amdgcn_raw_buffer_load_b128(rs, P1_OFF + (gi * 4 + m) * 1024 + lane * 16, 0, 16);
#pragma unroll
            for (int m = 0; m < 4; ++m) ok &= (pv[m][1] == T) & (pv[m][3] == T);
            if (__all(ok)) break;
            if (++spins > kSpinCap) break;
            __builtin_amdgcn_s_sleep(1);
        }
        g = (__uint_as_float(pv[0][0]) + __uint_as_float(pv[1][0])) + (__uint_as_float(pv[2][0]) + __uint_as_float(pv[3][0]));
        u = (__uint_as_float(pv[0][2]) + __uint_as_float(pv[1][2])) + (__uint_as_float(pv[2][2]) + __uint_as_float(pv[3][2]));
    }
    STAMP(4);
    unsigned hp[4];
    {
        const float gb = bf2f(f2bf(g)), ub = bf2f(f2bf(u));
        const float y2 = 1.5957691216057308f * (gb + 0.044715f * gb * gb * gb);
        const float ge = gb * __builtin_amdgcn_rcpf(1.0f + __expf(-y2));
        const unsigned hb = f2bf(bf2f(f2bf(ge)) * ub);
        const unsigned nb = dppu<0xB1>(hb);
        const unsigned P = __builtin_amdgcn_perm(nb, hb, (lane & 1) ? 0x01000504u : 0x05040100u);
        hp[0] = dppu<0x00>(P); hp[1] = dppu<0xAA>(P);
        hp[2] = dppu<0x141>(hp[0]); hp[3] = dppu<0x141>(hp[1]);
    }
    float o[8];
#pragma unroll
    for (int i = 0; i < 8; ++i) o[i] = 0.f;
#pragma unroll
    for (int t = 0; t < 4; ++t) {
#pragma unroll
        for (int k = 0; k < 4; ++k) {
            o[2 * k]     = dot2(plo(d0[t][k], d1[t][k]), hp[t], o[2 * k]);
            o[2 * k + 1] = dot2(phi(d0[t][k], d1[t][k]), hp[t], o[2 * k + 1]);
        }
    }
    {
        float y[4], z[2];
#pragma unroll
        for (int k = 0; k < 4; ++k) y[k] = tr32(o[k], o[k + 4]);
#pragma unroll
        for (int k = 0; k < 2; ++k) z[k] = tr16(y[k], y[k + 2]);
        const float q = tr8(z[0], z[1], bit3);
        const v2u p = {__float_as_uint(q), T};
        __builtin_amdgcn_raw_buffer_store_b64(p, rs, Q_OFF + gi * 8192 + (256 * gj + 64 * w + lane) * 8, 0, 16);
    }
    STAMP(5);
    if (b < 128 && w == 0) {
        const int cidx = (b & 7) * 16 + (b >> 3);
        const int pr = lane & 3, ps = lane >> 2;
        v4u val[4];
        const int base = Q_OFF + ps * 8192 + cidx * 64 + pr * 16;
        for (unsigned spins = 0;;) {
            asm volatile("" ::: "memory");
            bool ok = true;
#pragma unroll
            for (int k = 0; k < 4; ++k) {
                const int off = (k < 3) ? base + k * 16 * 8192 : base + ((ps < 8) ? 48 * 8192 : 0);
                val[k] = __builtin_amdgcn_raw_buffer_load_b128(rs, off, 0, 16);
            }
#pragma unroll
            for (int k = 0; k < 4; ++k) ok &= (val[k][1] == T) & (val[k][3] == T);
            if (__all(ok)) break;
            if (++spins > kSpinCap) break;
            __builtin_amdgcn_s_sleep(1);
        }
        STAMP(6);
        float s0 = (__uint_as_float(val[0][0]) + __uint_as_float(val[1][0])) + __uint_as_float(val[2][0]);
        float s1 = (__uint_as_float(val[0][2]) + __uint_as_float(val[1][2])) + __uint_as_float(val[2][2]);
        if (ps < 8) { s0 += __uint_as_float(val[3][0]); s1 += __uint_as_float(val[3][2]); }
        s0 += dppf<0x124>(s0); s1 += dppf<0x124>(s1);
        s0 += dppf<0x128>(s0); s1 += dppf<0x128>(s1);
        const float t16 = tr16(s0, s1);
        const float t32 = tr32(t16, t16);
        if ((lane & 0x2C) == 0) {
            const int k = 2 * pr + ((lane >> 4) & 1);
            out_[64 * (cidx >> 3) + 8 * k + (cidx & 7)] = (unsigned short)f2bf(t32);
        }
        if (b == 0 && lane == 0) *(GAS unsigned*)ws_ = T;
        STAMP(7);
    }
}

extern "C" void kernel_launch(void* const* d_in, const int* in_sizes, int n_in, void* d_out, int out_size, void* d_ws, size_t ws_size,
                              hipStream_t stream) {
    glu_kernel<<<NWG, 256, 0, stream>>>((const unsigned short*)d_in[0], (const unsigned short*)d_in[1], (const unsigned short*)d_in[2],
                                        (const unsigned short*)d_in[3], (unsigned short*)d_out, (unsigned*)d_ws);
}
```

```cpp
#include <hip/hip_runtime.h>
#include <hip/hip_bf16.h>

#define GAS __attribute__((address_space(1)))
typedef unsigned v4u __attribute__((ext_vector_type(4)));
typedef unsigned v2u __attribute__((ext_vector_type(2)));
typedef float v4f __attribute__((ext_vector_type(4)));
typedef __bf16 bf16x2 __attribute__((ext_vector_type(2)));

#define SLEEP1 20

namespace {
constexpr int DM = 1024, DF = 3584;
constexpr int NREG = 224;
constexpr int NWG = 252;
constexpr int P1_OFF = 4096;
constexpr int Q_OFF = P1_OFF + 56 * 8 * 1024;
constexpr int WS_USED = Q_OFF + 56 * 8192;
constexpr int OOB = (int)0x80000000u;
constexpr int AUX_NT = 2, AUX_SC1 = 16;
constexpr int RSRC3 = 0x00020000;

__device__ __forceinline__ float dot2(unsigned a, unsigned b, float c) {
    return __builtin_amdgcn_fdot2_f32_bf16(__builtin_bit_cast(bf16x2, a), __builtin_bit_cast(bf16x2, b), c, false);
}
__device__ __forceinline__ unsigned plo(unsigned r0, unsigned r1) { return __builtin_amdgcn_perm(r1, r0, 0x05040100u); }
__device__ __forceinline__ unsigned phi(unsigned r0, unsigned r1) { return __builtin_amdgcn_perm(r1, r0, 0x07060302u); }
__device__ __forceinline__ float bf2f(unsigned b) { return __uint_as_float(b << 16); }
__device__ __forceinline__ unsigned f2bf(float f) { __bf16 h = (__bf16)f; return (unsigned)__builtin_bit_cast(unsigned short, h); }
template <int CTRL> __device__ __forceinline__ float dppf(float v) {
    return __uint_as_float((unsigned)__builtin_amdgcn_update_dpp(0, (int)__float_as_uint(v), CTRL, 0xf, 0xf, true));
}
__device__ __forceinline__ float tr32(float a, float b) { auto r = __builtin_amdgcn_permlane32_swap(__float_as_uint(a), __float_as_uint(b), false, false); return __uint_as_float(r[0]) + __uint_as_float(r[1]); }
__device__ __forceinline__ float tr16(float a, float b) { auto r = __builtin_amdgcn_permlane16_swap(__float_as_uint(a), __float_as_uint(b), false, false); return __uint_as_float(r[0]) + __uint_as_float(r[1]); }
__device__ __forceinline__ float tr8(float a, float b, bool bit3) { const float a2 = a + dppf<0x128>(a), b2 = b + dppf<0x128>(b); return bit3 ? b2 : a2; }
#define LBAR() do { __builtin_amdgcn_sched_barrier(0); asm volatile("s_waitcnt lgkmcnt(0)\n\ts_barrier" ::: "memory"); __builtin_amdgcn_sched_barrier(0); } while (0)

#define GA_LD(B, B3, O)  "buffer_load_dwordx4 v[" #B ":" #B3 "], %[" #O "], %[rs], 0 offen sc1\n\t"
#define GA_INIT(R0, R1)  "v_mov_b32 v" #R0 ", %[i0]\n\tv_mov_b32 v" #R1 ", %[i1]\n\t"
#define GA_TAKE(W, B, B1, B2, B3, O, R0, R1) \
    "s_waitcnt vmcnt(" #W ")\n\t" \
    "v_cmp_eq_u32 vcc, %[T], v" #B1 "\n\t" \
    "v_cmp_eq_u32 %[m], %[T], v" #B3 "\n\t" \
    "s_and_b64 vcc, vcc, %[m]\n\t" \
    "v_cndmask_b32 v" #R0 ", v" #R0 ", v" #B ", vcc\n\t" \
    "v_cndmask_b32 v" #R1 ", v" #R1 ", v" #B2 ", vcc\n\t" \
    "v_cndmask_b32 %[" #O "], %[" #O "], %[s0], vcc\n\t"
#define GA_LD4  GA_LD(96, 99, o0) GA_LD(100, 103, o1) GA_LD(104, 107, o2) GA_LD(108, 111, o3)
#define GA_LD8  GA_LD4 GA_LD(112, 115, o4) GA_LD(116, 119, o5) GA_LD(120, 123, o6) GA_LD(124, 127, o7)
#define GA_TAIL(LOADS) \
    "v_cmp_gt_i32 vcc, 0, %[s1]\n\t" \
    "s_cmp_eq_u64 vcc, exec\n\t" \
    "s_cbranch_scc1 2f\n\t" \
    LOADS \
    "s_add_u32 %[c], %[c], 1\n\t" \
    "s_cmp_lt_u32 %[c], 0x10000\n\t" \
    "s_cbranch_scc1 1b\n\t" \
    "s_waitcnt vmcnt(0)\n\t" \
    "2:\n\t"

__device__ __forceinline__ void gather4(const __amdgpu_buffer_rsrc_t rs, int o0, int o1, int o2, int o3, float i0, float i1, unsigned T, float& s0, float& s1) {
    unsigned c; unsigned long long m;
    asm volatile(
        GA_LD4
        GA_INIT(128, 136) GA_INIT(129, 137) GA_INIT(130, 138) GA_INIT(131, 139)
        "v_bfrev_b32 %[s0], 1\n\t"
        "s_mov_b32 %[c], 0\n\t"
        "1:\n\t"
        GA_TAKE(3, 96, 97, 98, 99, o0, 128, 136)
        GA_TAKE(2, 100, 101, 102, 103, o1, 129, 137)
        GA_TAKE(1, 104, 105, 106, 107, o2, 130, 138)
        GA_TAKE(0, 108, 109, 110, 111, o3, 131, 139)
        "v_and_b32 %[s1], %[o0], %[o1]\n\t"
        "v_and_b32 %[s1], %[s1], %[o2]\n\t"
        "v_and_b32 %[s1], %[s1], %[o3]\n\t"
        GA_TAIL(GA_LD4)
        "v_add_f32 v128, v128, v129\n\tv_add_f32 v136, v136, v137\n\t"
        "v_add_f32 v130, v130, v131\n\tv_add_f32 v138, v138, v139\n\t"
        "v_add_f32 %[s0], v128, v130\n\tv_add_f32 %[s1], v136, v138\n\t"
        "s_nop 1"
        : [s0] "=&v"(s0), [s1] "=&v"(s1), [c] "=&s"(c), [m] "=&s"(m), [o0] "+v"(o0), [o1] "+v"(o1), [o2] "+v"(o2), [o3] "+v"(o3)
        : [i0] "v"(i0), [i1] "v"(i1), [T] "s"(T), [rs] "s"(rs)
        : "v96", "v97", "v98", "v99", "v100", "v101", "v102", "v103", "v104", "v105", "v106", "v107", "v108", "v109", "v110", "v111",
          "v128", "v129", "v130", "v131", "v136", "v137", "v138", "v139", "vcc", "scc", "memory");
}
__device__ __forceinline__ void gather8(const __amdgpu_buffer_rsrc_t rs, int o0, int o1, int o2, int o3, int o4, int o5, int o6, int o7, float i0, float i1, unsigned T, float& s0, float& s1) {
    unsigned c; unsigned long long m;
    asm volatile(
        GA_LD8
        GA_INIT(128, 136) GA_INIT(129, 137) GA_INIT(130, 138) GA_INIT(131, 139)
        GA_INIT(132, 140) GA_INIT(133, 141) GA_INIT(134, 142) GA_INIT(135, 143)
        "v_bfrev_b32 %[s0], 1\n\t"
        "s_mov_b32 %[c], 0\n\t"
        "1:\n\t"
        GA_TAKE(7, 96, 97, 98, 99, o0, 128, 136)
        GA_TAKE(6, 100, 101, 102, 103, o1, 129, 137)
        GA_TAKE(5, 104, 105, 106, 107, o2, 130, 138)
        GA_TAKE(4, 108, 109, 110, 111, o3, 131, 139)
        GA_TAKE(3, 112, 113, 114, 115, o4, 132, 140)
        GA_TAKE(2, 116, 117, 118, 119, o5, 133, 141)
        GA_TAKE(1, 120, 121, 122, 123, o6, 134, 142)
        GA_TAKE(0, 124, 125, 126, 127, o7, 135, 143)
        "v_and_b32 %[s1], %[o0], %[o1]\n\t"
        "v_and_b32 %[s1], %[s1], %[o2]\n\t"
        "v_and_b32 %[s1], %[s1], %[o3]\n\t"
        "v_and_b32 %[s1], %[s1], %[o4]\n\t"
        "v_and_b32 %[s1], %[s1], %[o5]\n\t"
        "v_and_b32 %[s1], %[s1], %[o6]\n\t"
        "v_and_b32 %[s1], %[s1], %[o7]\n\t"
        GA_TAIL(GA_LD8)
        "v_add_f32 v128, v128, v129\n\tv_add_f32 v136, v136, v137\n\t"
        "v_add_f32 v130, v130, v131\n\tv_add_f32 v138, v138, v139\n\t"
        "v_add_f32 v132, v132, v133\n\tv_add_f32 v140, v140, v141\n\t"
        "v_add_f32 v134, v134, v135\n\tv_add_f32 v142, v142, v143\n\t"
        "v_add_f32 v128, v128, v130\n\tv_add_f32 v136, v136, v138\n\t"
        "v_add_f32 v132, v132, v134\n\tv_add_f32 v140, v140, v142\n\t"
        "v_add_f32 %[s0], v128, v132\n\tv_add_f32 %[s1], v136, v140\n\t"
        "s_nop 1"
        : [s0] "=&v"(s0), [s1] "=&v"(s1), [c] "=&s"(c), [m] "=&s"(m), [o0] "+v"(o0), [o1] "+v"(o1), [o2] "+v"(o2), [o3] "+v"(o3),
          [o4] "+v"(o4), [o5] "+v"(o5), [o6] "+v"(o6), [o7] "+v"(o7)
        : [i0] "v"(i0), [i1] "v"(i1), [T] "s"(T), [rs] "s"(rs)
        : "v96", "v97", "v98", "v99", "v100", "v101", "v102", "v103", "v104", "v105", "v106", "v107", "v108", "v109", "v110", "v111",
          "v112", "v113", "v114", "v115", "v116", "v117", "v118", "v119", "v120", "v121", "v122", "v123", "v124", "v125", "v126", "v127",
          "v128", "v129", "v130", "v131", "v132", "v133", "v134", "v135", "v136", "v137", "v138", "v139", "v140", "v141", "v142", "v143",
          "vcc", "scc", "memory");
}
}

__global__ void __launch_bounds__(320, 2)
glu_kernel(const unsigned short* __restrict__ x_, const unsigned short* __restrict__ wg_, const unsigned short* __restrict__ wu_,
           const unsigned short* __restrict__ wd_, unsigned short* __restrict__ out_, unsigned* __restrict__ ws_) {
    __shared__ __attribute__((aligned(16))) float red[4 * 2 * 64];
    __shared__ __attribute__((aligned(16))) unsigned hs[36];
    const int b = blockIdx.x, tid = threadIdx.x, lane = tid & 63;
    const int w = __builtin_amdgcn_readfirstlane(tid >> 6);
    asm volatile("" :: "s"(x_), "s"(wg_), "s"(wu_), "s"(wd_), "s"(out_), "s"(ws_));
    const __amdgpu_buffer_rsrc_t rs = __builtin_amdgcn_make_buffer_rsrc((void*)ws_, 0, WS_USED, RSRC3);
    const int s = (b & 7) * 28 + (b >> 3);
    const int cls = (3 - (int)((unsigned long long)wg_ >> 7)) & 7;
    const bool helper = b >= NREG;
    const int gi = helper ? cls + 8 * ((b - NREG) >> 2) : s >> 2;
    const int gj = helper ? (b - NREG) & 3 : s & 3;
    const bool slow = (gi & 7) == cls;
    const bool bit3 = (lane & 8) != 0;

    if (w >= 4) {
        const unsigned E = __builtin_amdgcn_raw_buffer_load_b32(rs, 0, 0, AUX_SC1);
        const int pbase = P1_OFF + gi * 8192 + lane * 16;
        int poff = pbase + (helper ? 4 + gj : gj) * 1024;
        int off[8];
#pragma unroll
        for (int m = 0; m < 8; ++m) off[m] = (m == gj || (m >= 4 && !slow)) ? OOB : pbase + m * 1024;
        const int cidx = (b & 7) * 16 + (b >> 3);
        const int pr = lane & 3, ps = lane >> 2;
        int off2[4];
#pragma unroll
        for (int k = 0; k < 4; ++k) off2[k] = ((k == 3) && (ps >= 8)) ? OOB : Q_OFF + (16 * k + ps) * 8192 + cidx * 64 + pr * 16;
        int ooff = (64 * (cidx >> 3) + 8 * (2 * pr + ((lane >> 4) & 1)) + (cidx & 7)) * 2;
        int rd0 = lane * 4;
        asm volatile("" : "+v"(poff), "+v"(off[0]), "+v"(off[1]), "+v"(off[2]), "+v"(off[3]), "+v"(off[4]), "+v"(off[5]), "+v"(off[6]), "+v"(off[7]));
        asm volatile("" : "+v"(off2[0]), "+v"(off2[1]), "+v"(off2[2]), "+v"(off2[3]), "+v"(ooff), "+v"(rd0));
        unsigned T = __builtin_amdgcn_readfirstlane(max(E + 1u, 1u));
        asm volatile("" : "+s"(T));
        if (lane == 0) hs[32] = T;
        LBAR();
        const float* rp = (const float*)((const unsigned char*)red + rd0);
        const float og = (rp[0 * 64] + rp[2 * 64]) + (rp[4 * 64] + rp[6 * 64]);
        const float ou = (rp[1 * 64] + rp[3 * 64]) + (rp[5 * 64] + rp[7 * 64]);
        {
            const v4u p = {__float_as_uint(og), T, __float_as_uint(ou), T};
            __builtin_amdgcn_raw_buffer_store_b128(p, rs, poff, 0, AUX_SC1);
        }
        if (helper) return;
        __builtin_amdgcn_s_sleep(SLEEP1);
        float g, u;
        if (slow) gather8(rs, off[0], off[1], off[2], off[3], off[4], off[5], off[6], off[7], og, ou, T, g, u);
        else gather4(rs, off[0], off[1], off[2], off[3], og, ou, T, g, u);
        {
            const float gb = bf2f(f2bf(g)), ub = bf2f(f2bf(u));
            const float e2 = __builtin_amdgcn_exp2f(gb * __builtin_fmaf(gb * gb, -0.10294324f, -2.3022082f));
            const float ge = gb * __builtin_amdgcn_rcpf(1.0f + e2);
            ((unsigned short*)hs)[lane] = (unsigned short)f2bf(bf2f(f2bf(ge)) * ub);
        }
        LBAR();
        if (b < 128) {
            float s0, s1;
            gather4(rs, off2[0], off2[1], off2[2], off2[3], 0.f, 0.f, T, s0, s1);
            s0 += dppf<0x124>(s0); s1 += dppf<0x124>(s1);
            s0 += dppf<0x128>(s0); s1 += dppf<0x128>(s1);
            const float t16 = tr16(s0, s1);
            const float t32 = tr32(t16, t16);
            if ((lane & 0x2C) == 0) *(GAS unsigned short*)((GAS unsigned char*)out_ + ooff) = (unsigned short)f2bf(t32);
            if (b == 0 && lane == 0) *(GAS unsigned*)ws_ = T;
        }
    } else {
        const __amdgpu_buffer_rsrc_t rg = __builtin_amdgcn_make_buffer_rsrc((void*)wg_, 0, DM * DF * 2, RSRC3);
        const __amdgpu_buffer_rsrc_t ru = __builtin_amdgcn_make_buffer_rsrc((void*)wu_, 0, DM * DF * 2, RSRC3);
        const __amdgpu_buffer_rsrc_t rd = __builtin_amdgcn_make_buffer_rsrc((void*)wd_, 0, DM * DF * 2, RSRC3);
        const int r = lane >> 3, cg = lane & 7;
        const int row0 = 256 * gj + 64 * w;
        const v4u xp = *(const GAS v4u*)((const GAS unsigned char*)x_ + (row0 + 8 * r) * 2);
        const int gv = (8 * r) * (DF * 2) + cg * 16;
        const int gvA = helper ? OOB : gv, gvB = (slow && !helper) ? OOB : gv;
        const int gso = row0 * (DF * 2) + 128 * gi;
        v4u g0[4], g1[4], u0[4], u1[4];
#pragma unroll
        for (int t = 0; t < 4; ++t) {
            const int v = t < 2 ? gvA : gvB;
            g0[t] = __builtin_amdgcn_raw_buffer_load_b128(rg, v, gso + (2 * t) * (DF * 2), AUX_NT);
            g1[t] = __builtin_amdgcn_raw_buffer_load_b128(rg, v, gso + (2 * t + 1) * (DF * 2), AUX_NT);
            u0[t] = __builtin_amdgcn_raw_buffer_load_b128(ru, v, gso + (2 * t) * (DF * 2), AUX_NT);
            u1[t] = __builtin_amdgcn_raw_buffer_load_b128(ru, v, gso + (2 * t + 1) * (DF * 2), AUX_NT);
        }
        __builtin_amdgcn_sched_barrier(0);
        const int dv = helper ? OOB : (8 * r) * (DM * 2) + cg * 16;
        const int dso = (64 * gi) * (DM * 2) + 512 * gj + 128 * w;
        float a[16];
#pragma unroll
        for (int i = 0; i < 16; ++i) a[i] = 0.f;
#pragma unroll
        for (int t = 0; t < 4; ++t) {
            const unsigned xx = xp[t];
#pragma unroll
            for (int k = 0; k < 4; ++k) {
                a[2 * k]         = dot2(plo(g0[t][k], g1[t][k]), xx, a[2 * k]);
                a[2 * k + 1]     = dot2(phi(g0[t][k], g1[t][k]), xx, a[2 * k + 1]);
                a[8 + 2 * k]     = dot2(plo(u0[t][k], u1[t][k]), xx, a[8 + 2 * k]);
                a[8 + 2 * k + 1] = dot2(phi(u0[t][k], u1[t][k]), xx, a[8 + 2 * k + 1]);
            }
        }
        asm volatile("" : "+v"(a[0]), "+v"(a[1]), "+v"(a[2]), "+v"(a[3]), "+v"(a[4]), "+v"(a[5]), "+v"(a[6]), "+v"(a[7]));
        asm volatile("" : "+v"(a[8]), "+v"(a[9]), "+v"(a[10]), "+v"(a[11]), "+v"(a[12]), "+v"(a[13]), "+v"(a[14]), "+v"(a[15]));
        __builtin_amdgcn_sched_barrier(0);
        v4u d0[4], d1[4];
#pragma unroll
        for (int t = 0; t < 4; ++t) {
            d0[t] = __builtin_amdgcn_raw_buffer_load_b128(rd, dv, dso + (2 * t) * 2048, AUX_NT);
            d1[t] = __builtin_amdgcn_raw_buffer_load_b128(rd, dv + 2048, dso + (2 * t) * 2048, AUX_NT);
        }
        __builtin_amdgcn_sched_barrier(0);
        {
            float y[8], z[4];
#pragma unroll
            for (int k = 0; k < 8; ++k) y[k] = tr32(a[k], a[k + 8]);
#pragma unroll
            for (int k = 0; k < 4; ++k) z[k] = tr16(y[k], y[k + 4]);
            float2 q; q.x = tr8(z[0], z[2], bit3); q.y = tr8(z[1], z[3], bit3);
            const int col0 = 8 * cg + 4 * ((lane >> 4) & 1) + 2 * ((lane >> 3) & 1);
            *(float2*)&red[(w * 2 + (lane >> 5)) * 64 + col0] = q;
        }
        LBAR();
        if (helper) return;
        unsigned dl[4][4], dh[4][4];
#pragma unroll
        for (int t = 0; t < 4; ++t)
#pragma unroll
            for (int k = 0; k < 4; ++k) { dl[t][k] = plo(d0[t][k], d1[t][k]); dh[t][k] = phi(d0[t][k], d1[t][k]); }
        int qoff = Q_OFF + gi * 8192 + (256 * gj + 64 * w + lane) * 8;
        unsigned T = hs[32];
#pragma unroll
        for (int t = 0; t < 4; ++t) asm volatile("" : "+v"(dl[t][0]), "+v"(dl[t][1]), "+v"(dl[t][2]), "+v"(dl[t][3]), "+v"(dh[t][0]), "+v"(dh[t][1]), "+v"(dh[t][2]), "+v"(dh[t][3]));
        asm volatile("" : "+v"(T), "+v"(qoff));
        LBAR();
        const v4u hp = *(const v4u*)&hs[4 * r];
        float o[8];
#pragma unroll
        for (int i = 0; i < 8; ++i) o[i] = 0.f;
#pragma unroll
        for (int t = 0; t < 4; ++t) {
            const unsigned hh = hp[t];
#pragma unroll
            for (int k = 0; k < 4; ++k) {
                o[2 * k]     = dot2(dl[t][k], hh, o[2 * k]);
                o[2 * k + 1] = dot2(dh[t][k], hh, o[2 * k + 1]);
            }
        }
        float y[4], z[2];
#pragma unroll
        for (int k = 0; k < 4; ++k) y[k] = tr32(o[k], o[k + 4]);
#pragma unroll
        for (int k = 0; k < 2; ++k) z[k] = tr16(y[k], y[k + 2]);
        const float q = tr8(z[0], z[1], bit3);
        const v2u p = {__float_as_uint(q), T};
        __builtin_amdgcn_raw_buffer_store_b64(p, rs, qoff, 0, AUX_SC1);
    }
}

extern "C" void kernel_launch(void* const* d_in, const int* in_sizes, int n_in, void* d_out, int out_size, void* d_ws, size_t ws_size,
                              hipStream_t stream) {
    glu_kernel<<<NWG, 320, 0, stream>>>((const unsigned short*)d_in[0], (const unsigned short*)d_in[1], (const unsigned short*)d_in[2],
                                        (const unsigned short*)d_in[3], (unsigned short*)d_out, (unsigned*)d_ws);
}
```

```cpp
#include <hip/hip_runtime.h>
#include <hip/hip_bf16.h>

#define GAS __attribute__((address_space(1)))
typedef unsigned v4u __attribute__((ext_vector_type(4)));
typedef unsigned v2u __attribute__((ext_vector_type(2)));
typedef float v4f __attribute__((ext_vector_type(4)));
typedef __bf16 bf16x2 __attribute__((ext_vector_type(2)));

#define PFCODE 1
#define PFBACK 2048
#define PFLINES 40
#define SLEEP1 20

namespace {
constexpr int DM = 1024, DF = 3584;
constexpr int NREG = 224;
constexpr int NWG = 252;
constexpr int P1_OFF = 4096;
constexpr int Q_OFF = P1_OFF + 56 * 8 * 1024;
constexpr int WS_USED = Q_OFF + 56 * 8192;
constexpr int OOB = (int)0x80000000u;
constexpr int AUX_NT = 2, AUX_SC1 = 16;
constexpr int RSRC3 = 0x00020000;

__device__ __forceinline__ float dot2(unsigned a, unsigned b, float c) {
    return __builtin_amdgcn_fdot2_f32_bf16(__builtin_bit_cast(bf16x2, a), __builtin_bit_cast(bf16x2, b), c, false);
}
__device__ __forceinline__ unsigned plo(unsigned r0, unsigned r1) { return __builtin_amdgcn_perm(r1, r0, 0x05040100u); }
__device__ __forceinline__ unsigned phi(unsigned r0, unsigned r1) { return __builtin_amdgcn_perm(r1, r0, 0x07060302u); }
__device__ __forceinline__ float bf2f(unsigned b) { return __uint_as_float(b << 16); }
__device__ __forceinline__ unsigned f2bf(float f) { __bf16 h = (__bf16)f; return (unsigned)__builtin_bit_cast(unsigned short, h); }
template <int CTRL> __device__ __forceinline__ float dppf(float v) {
    return __uint_as_float((unsigned)__builtin_amdgcn_update_dpp(0, (int)__float_as_uint(v), CTRL, 0xf, 0xf, true));
}
__device__ __forceinline__ float tr32(float a, float b) { auto r = __builtin_amdgcn_permlane32_swap(__float_as_uint(a), __float_as_uint(b), false, false); return __uint_as_float(r[0]) + __uint_as_float(r[1]); }
__device__ __forceinline__ float tr16(float a, float b) { auto r = __builtin_amdgcn_permlane16_swap(__float_as_uint(a), __float_as_uint(b), false, false); return __uint_as_float(r[0]) + __uint_as_float(r[1]); }
__device__ __forceinline__ float tr8(float a, float b, bool bit3) { const float a2 = a + dppf<0x128>(a), b2 = b + dppf<0x128>(b); return bit3 ? b2 : a2; }
#define LBAR() do { __builtin_amdgcn_sched_barrier(0); asm volatile("s_waitcnt lgkmcnt(0)\n\ts_barrier" ::: "memory"); __builtin_amdgcn_sched_barrier(0); } while (0)

#define GA_LD(B, B3, O)  "buffer_load_dwordx4 v[" #B ":" #B3 "], %[" #O "], %[rs], 0 offen sc1\n\t"
#define GA_INIT(R0, R1)  "v_mov_b32 v" #R0 ", %[i0]\n\tv_mov_b32 v" #R1 ", %[i1]\n\t"
#define GA_TAKE(W, B, B1, B2, B3, O, R0, R1) \
    "s_waitcnt vmcnt(" #W ")\n\t" \
    "v_cmp_eq_u32 vcc, %[T], v" #B1 "\n\t" \
    "v_cmp_eq_u32 %[m], %[T], v" #B3 "\n\t" \
    "s_and_b64 vcc, vcc, %[m]\n\t" \
    "v_cndmask_b32 v" #R0 ", v" #R0 ", v" #B ", vcc\n\t" \
    "v_cndmask_b32 v" #R1 ", v" #R1 ", v" #B2 ", vcc\n\t" \
    "v_cndmask_b32 %[" #O "], %[" #O "], %[s0], vcc\n\t"
#define GA_LD4  GA_LD(96, 99, o0) GA_LD(100, 103, o1) GA_LD(104, 107, o2) GA_LD(108, 111, o3)
#define GA_LD8  GA_LD4 GA_LD(112, 115, o4) GA_LD(116, 119, o5) GA_LD(120, 123, o6) GA_LD(124, 127, o7)
#define GA_TAIL(LOADS) \
    "v_cmp_gt_i32 vcc, 0, %[s1]\n\t" \
    "s_cmp_eq_u64 vcc, exec\n\t" \
    "s_cbranch_scc1 2f\n\t" \
    LOADS \
    "s_add_u32 %[c], %[c], 1\n\t" \
    "s_cmp_lt_u32 %[c], 0x10000\n\t" \
    "s_cbranch_scc1 1b\n\t" \
    "s_waitcnt vmcnt(0)\n\t" \
    "2:\n\t"

__device__ __forceinline__ void gather4(const __amdgpu_buffer_rsrc_t rs, int o0, int o1, int o2, int o3, float i0, float i1, unsigned T, float& s0, float& s1) {
    unsigned c; unsigned long long m;
    asm volatile(
        GA_LD4
        GA_INIT(128, 136) GA_INIT(129, 137) GA_INIT(130, 138) GA_INIT(131, 139)
        "v_bfrev_b32 %[s0], 1\n\t"
        "s_mov_b32 %[c], 0\n\t"
        "1:\n\t"
        GA_TAKE(3, 96, 97, 98, 99, o0, 128, 136)
        GA_TAKE(2, 100, 101, 102, 103, o1, 129, 137)
        GA_TAKE(1, 104, 105, 106, 107, o2, 130, 138)
        GA_TAKE(0, 108, 109, 110, 111, o3, 131, 139)
        "v_and_b32 %[s1], %[o0], %[o1]\n\t"
        "v_and_b32 %[s1], %[s1], %[o2]\n\t"
        "v_and_b32 %[s1], %[s1], %[o3]\n\t"
        GA_TAIL(GA_LD4)
        "v_add_f32 v128, v128, v129\n\tv_add_f32 v136, v136, v137\n\t"
        "v_add_f32 v130, v130, v131\n\tv_add_f32 v138, v138, v139\n\t"
        "v_add_f32 %[s0], v128, v130\n\tv_add_f32 %[s1], v136, v138\n\t"
        "s_nop 1"
        : [s0] "=&v"(s0), [s1] "=&v"(s1), [c] "=&s"(c), [m] "=&s"(m), [o0] "+v"(o0), [o1] "+v"(o1), [o2] "+v"(o2), [o3] "+v"(o3)
        : [i0] "v"(i0), [i1] "v"(i1), [T] "s"(T), [rs] "s"(rs)
        : "v96", "v97", "v98", "v99", "v100", "v101", "v102", "v103", "v104", "v105", "v106", "v107", "v108", "v109", "v110", "v111",
          "v128", "v129", "v130", "v131", "v136", "v137", "v138", "v139", "vcc", "scc", "memory");
}
__device__ __forceinline__ void gather8(const __amdgpu_buffer_rsrc_t rs, int o0, int o1, int o2, int o3, int o4, int o5, int o6, int o7, float i0, float i1, unsigned T, float& s0, float& s1) {
    unsigned c; unsigned long long m;
    asm volatile(
        GA_LD8
        GA_INIT(128, 136) GA_INIT(129, 137) GA_INIT(130, 138) GA_INIT(131, 139)
        GA_INIT(132, 140) GA_INIT(133, 141) GA_INIT(134, 142) GA_INIT(135, 143)
        "v_bfrev_b32 %[s0], 1\n\t"
        "s_mov_b32 %[c], 0\n\t"
        "1:\n\t"
        GA_TAKE(7, 96, 97, 98, 99, o0, 128, 136)
        GA_TAKE(6, 100, 101, 102, 103, o1, 129, 137)
        GA_TAKE(5, 104, 105, 106, 107, o2, 130, 138)
        GA_TAKE(4, 108, 109, 110, 111, o3, 131, 139)
        GA_TAKE(3, 112, 113, 114, 115, o4, 132, 140)
        GA_TAKE(2, 116, 117, 118, 119, o5, 133, 141)
        GA_TAKE(1, 120, 121, 122, 123, o6, 134, 142)
        GA_TAKE(0, 124, 125, 126, 127, o7, 135, 143)
        "v_and_b32 %[s1], %[o0], %[o1]\n\t"
        "v_and_b32 %[s1], %[s1], %[o2]\n\t"
        "v_and_b32 %[s1], %[s1], %[o3]\n\t"
        "v_and_b32 %[s1], %[s1], %[o4]\n\t"
        "v_and_b32 %[s1], %[s1], %[o5]\n\t"
        "v_and_b32 %[s1], %[s1], %[o6]\n\t"
        "v_and_b32 %[s1], %[s1], %[o7]\n\t"
        GA_TAIL(GA_LD8)
        "v_add_f32 v128, v128, v129\n\tv_add_f32 v136, v136, v137\n\t"
        "v_add_f32 v130, v130, v131\n\tv_add_f32 v138, v138, v139\n\t"
        "v_add_f32 v132, v132, v133\n\tv_add_f32 v140, v140, v141\n\t"
        "v_add_f32 v134, v134, v135\n\tv_add_f32 v142, v142, v143\n\t"
        "v_add_f32 v128, v128, v130\n\tv_add_f32 v136, v136, v138\n\t"
        "v_add_f32 v132, v132, v134\n\tv_add_f32 v140, v140, v142\n\t"
        "v_add_f32 %[s0], v128, v132\n\tv_add_f32 %[s1], v136, v140\n\t"
        "s_nop 1"
        : [s0] "=&v"(s0), [s1] "=&v"(s1), [c] "=&s"(c), [m] "=&s"(m), [o0] "+v"(o0), [o1] "+v"(o1), [o2] "+v"(o2), [o3] "+v"(o3),
          [o4] "+v"(o4), [o5] "+v"(o5), [o6] "+v"(o6), [o7] "+v"(o7)
        : [i0] "v"(i0), [i1] "v"(i1), [T] "s"(T), [rs] "s"(rs)
        : "v96", "v97", "v98", "v99", "v100", "v101", "v102", "v103", "v104", "v105", "v106", "v107", "v108", "v109", "v110", "v111",
          "v112", "v113", "v114", "v115", "v116", "v117", "v118", "v119", "v120", "v121", "v122", "v123", "v124", "v125", "v126", "v127",
          "v128", "v129", "v130", "v131", "v132", "v133", "v134", "v135", "v136", "v137", "v138", "v139", "v140", "v141", "v142", "v143",
          "vcc", "scc", "memory");
}
}

__global__ void __launch_bounds__(320, 2)
glu_kernel(const unsigned short* __restrict__ x_, const unsigned short* __restrict__ wg_, const unsigned short* __restrict__ wu_,
           const unsigned short* __restrict__ wd_, unsigned short* __restrict__ out_, unsigned* __restrict__ ws_) {
    __shared__ __attribute__((aligned(16))) float red[4 * 2 * 64];
    __shared__ __attribute__((aligned(16))) unsigned hs[36];
    const int b = blockIdx.x, tid = threadIdx.x, lane = tid & 63;
    const int w = __builtin_amdgcn_readfirstlane(tid >> 6);
    asm volatile("" :: "s"(x_), "s"(wg_), "s"(wu_), "s"(wd_), "s"(out_), "s"(ws_));
    const __amdgpu_buffer_rsrc_t rs = __builtin_amdgcn_make_buffer_rsrc((void*)ws_, 0, WS_USED, RSRC3);
    const int s = (b & 7) * 28 + (b >> 3);
    const int cls = (3 - (int)((unsigned long long)wg_ >> 7)) & 7;
    const bool helper = b >= NREG;
    const int gi = helper ? cls + 8 * ((b - NREG) >> 2) : s >> 2;
    const int gj = helper ? (b - NREG) & 3 : s & 3;
    const bool slow = (gi & 7) == cls;
    const bool bit3 = (lane & 8) != 0;

    if (w >= 4) {
        const unsigned E = __builtin_amdgcn_raw_buffer_load_b32(rs, 0, 0, AUX_SC1);
        unsigned pfc;
        {
            unsigned long long pc; asm volatile("s_getpc_b64 %0" : "=s"(pc));
            const GAS unsigned char* cp = (const GAS unsigned char*)((pc & ~127ull) - PFBACK) + min(lane, PFLINES - 1) * 128;
            pfc = *(const GAS unsigned*)cp;
        }
        const int pbase = P1_OFF + gi * 8192 + lane * 16;
        int poff = pbase + (helper ? 4 + gj : gj) * 1024;
        int off[8];
#pragma unroll
        for (int m = 0; m < 8; ++m) off[m] = (m == gj || (m >= 4 && !slow)) ? OOB : pbase + m * 1024;
        const int cidx = (b & 7) * 16 + (b >> 3);
        const int pr = lane & 3, ps = lane >> 2;
        int off2[4];
#pragma unroll
        for (int k = 0; k < 4; ++k) off2[k] = ((k == 3) && (ps >= 8)) ? OOB : Q_OFF + (16 * k + ps) * 8192 + cidx * 64 + pr * 16;
        int ooff = (64 * (cidx >> 3) + 8 * (2 * pr + ((lane >> 4) & 1)) + (cidx & 7)) * 2;
        int rd0 = lane * 4;
        asm volatile("" : "+v"(poff), "+v"(off[0]), "+v"(off[1]), "+v"(off[2]), "+v"(off[3]), "+v"(off[4]), "+v"(off[5]), "+v"(off[6]), "+v"(off[7]));
        asm volatile("" : "+v"(off2[0]), "+v"(off2[1]), "+v"(off2[2]), "+v"(off2[3]), "+v"(ooff), "+v"(rd0));
        unsigned T = __builtin_amdgcn_readfirstlane(max(E + 1u, 1u));
        asm volatile("" : "+s"(T));
        if (lane == 0) hs[32] = T;
        asm volatile("" :: "v"(pfc));
        LBAR();
        const float* rp = (const float*)((const unsigned char*)red + rd0);
        const float og = (rp[0 * 64] + rp[2 * 64]) + (rp[4 * 64] + rp[6 * 64]);
        const float ou = (rp[1 * 64] + rp[3 * 64]) + (rp[5 * 64] + rp[7 * 64]);
        {
            const v4u p = {__float_as_uint(og), T, __float_as_uint(ou), T};
            __builtin_amdgcn_raw_buffer_store_b128(p, rs, poff, 0, AUX_SC1);
        }
        if (helper) return;
        __builtin_amdgcn_s_sleep(SLEEP1);
        float g, u;
        if (slow) gather8(rs, off[0], off[1], off[2], off[3], off[4], off[5], off[6], off[7], og, ou, T, g, u);
        else gather4(rs, off[0], off[1], off[2], off[3], og, ou, T, g, u);
        {
            const float gb = bf2f(f2bf(g)), ub = bf2f(f2bf(u));
            const float e2 = __builtin_amdgcn_exp2f(gb * __builtin_fmaf(gb * gb, -0.10294324f, -2.3022082f));
            const float ge = gb * __builtin_amdgcn_rcpf(1.0f + e2);
            ((unsigned short*)hs)[lane] = (unsigned short)f2bf(bf2f(f2bf(ge)) * ub);
        }
        LBAR();
        if (b < 128) {
            float s0, s1;
            gather4(rs, off2[0], off2[1], off2[2], off2[3], 0.f, 0.f, T, s0, s1);
            s0 += dppf<0x124>(s0); s1 += dppf<0x124>(s1);
            s0 += dppf<0x128>(s0); s1 += dppf<0x128>(s1);
            const float t16 = tr16(s0, s1);
            const float t32 = tr32(t16, t16);
            if ((lane & 0x2C) == 0) *(GAS unsigned short*)((GAS unsigned char*)out_ + ooff) = (unsigned short)f2bf(t32);
            if (b == 0 && lane == 0) *(GAS unsigned*)ws_ = T;
        }
    } else {
        const __amdgpu_buffer_rsrc_t rg = __builtin_amdgcn_make_buffer_rsrc((void*)wg_, 0, DM * DF * 2, RSRC3);
        const __amdgpu_buffer_rsrc_t ru = __builtin_amdgcn_make_buffer_rsrc((void*)wu_, 0, DM * DF * 2, RSRC3);
        const __amdgpu_buffer_rsrc_t rd = __builtin_amdgcn_make_buffer_rsrc((void*)wd_, 0, DM * DF * 2, RSRC3);
        const int r = lane >> 3, cg = lane & 7;
        const int row0 = 256 * gj + 64 * w;
        const v4u xp = *(const GAS v4u*)((const GAS unsigned char*)x_ + (row0 + 8 * r) * 2);
        const int gv = (8 * r) * (DF * 2) + cg * 16;
        const int gvA = helper ? OOB : gv, gvB = (slow && !helper) ? OOB : gv;
        const int gso = row0 * (DF * 2) + 128 * gi;
        v4u g0[4], g1[4], u0[4], u1[4];
#pragma unroll
        for (int t = 0; t < 4; ++t) {
            const int v = t < 2 ? gvA : gvB;
            g0[t] = __builtin_amdgcn_raw_buffer_load_b128(rg, v, gso + (2 * t) * (DF * 2), AUX_NT);
            g1[t] = __builtin_amdgcn_raw_buffer_load_b128(rg, v, gso + (2 * t + 1) * (DF * 2), AUX_NT);
            u0[t] = __builtin_amdgcn_raw_buffer_load_b128(ru, v, gso + (2 * t) * (DF * 2), AUX_NT);
            u1[t] = __builtin_amdgcn_raw_buffer_load_b128(ru, v, gso + (2 * t + 1) * (DF * 2), AUX_NT);
        }
        __builtin_amdgcn_sched_barrier(0);
        const int dv = helper ? OOB : (8 * r) * (DM * 2) + cg * 16;
        const int dso = (64 * gi) * (DM * 2) + 512 * gj + 128 * w;
        float a[16];
#pragma unroll
        for (int i = 0; i < 16; ++i) a[i] = 0.f;
#pragma unroll
        for (int t = 0; t < 4; ++t) {
            const unsigned xx = xp[t];
#pragma unroll
            for (int k = 0; k < 4; ++k) {
                a[2 * k]         = dot2(plo(g0[t][k], g1[t][k]), xx, a[2 * k]);
                a[2 * k + 1]     = dot2(phi(g0[t][k], g1[t][k]), xx, a[2 * k + 1]);
                a[8 + 2 * k]     = dot2(plo(u0[t][k], u1[t][k]), xx, a[8 + 2 * k]);
                a[8 + 2 * k + 1] = dot2(phi(u0[t][k], u1[t][k]), xx, a[8 + 2 * k + 1]);
            }
        }
        asm volatile("" : "+v"(a[0]), "+v"(a[1]), "+v"(a[2]), "+v"(a[3]), "+v"(a[4]), "+v"(a[5]), "+v"(a[6]), "+v"(a[7]));
        asm volatile("" : "+v"(a[8]), "+v"(a[9]), "+v"(a[10]), "+v"(a[11]), "+v"(a[12]), "+v"(a[13]), "+v"(a[14]), "+v"(a[15]));
        __builtin_amdgcn_sched_barrier(0);
        v4u d0[4], d1[4];
#pragma unroll
        for (int t = 0; t < 4; ++t) {
            d0[t] = __builtin_amdgcn_raw_buffer_load_b128(rd, dv, dso + (2 * t) * 2048, AUX_NT);
            d1[t] = __builtin_amdgcn_raw_buffer_load_b128(rd, dv + 2048, dso + (2 * t) * 2048, AUX_NT);
        }
        __builtin_amdgcn_sched_barrier(0);
        {
            float y[8], z[4];
#pragma unroll
            for (int k = 0; k < 8; ++k) y[k] = tr32(a[k], a[k + 8]);
#pragma unroll
            for (int k = 0; k < 4; ++k) z[k] = tr16(y[k], y[k + 4]);
            float2 q; q.x = tr8(z[0], z[2], bit3); q.y = tr8(z[1], z[3], bit3);
            const int col0 = 8 * cg + 4 * ((lane >> 4) & 1) + 2 * ((lane >> 3) & 1);
            *(float2*)&red[(w * 2 + (lane >> 5)) * 64 + col0] = q;
        }
        LBAR();
        if (helper) return;
        unsigned dl[4][4], dh[4][4];
#pragma unroll
        for (int t = 0; t < 4; ++t)
#pragma unroll
            for (int k = 0; k < 4; ++k) { dl[t][k] = plo(d0[t][k], d1[t][k]); dh[t][k] = phi(d0[t][k], d1[t][k]); }
        int qoff = Q_OFF + gi * 8192 + (256 * gj + 64 * w + lane) * 8;
        unsigned T = hs[32];
#pragma unroll
        for (int t = 0; t < 4; ++t) asm volatile("" : "+v"(dl[t][0]), "+v"(dl[t][1]), "+v"(dl[t][2]), "+v"(dl[t][3]), "+v"(dh[t][0]), "+v"(dh[t][1]), "+v"(dh[t][2]), "+v"(dh[t][3]));
        asm volatile("" : "+v"(T), "+v"(qoff));
        LBAR();
        const v4u hp = *(const v4u*)&hs[4 * r];
        float o[8];
#pragma unroll
        for (int i = 0; i < 8; ++i) o[i] = 0.f;
#pragma unroll
        for (int t = 0; t < 4; ++t) {
            const unsigned hh = hp[t];
#pragma unroll
            for (int k = 0; k < 4; ++k) {
                o[2 * k]     = dot2(dl[t][k], hh, o[2 * k]);
                o[2 * k + 1] = dot2(dh[t][k], hh, o[2 * k + 1]);
            }
        }
        float y[4], z[2];
#pragma unroll
        for (int k = 0; k < 4; ++k) y[k] = tr32(o[k], o[k + 4]);
#pragma unroll
        for (int k = 0; k < 2; ++k) z[k] = tr16(y[k], y[k + 2]);
        const float q = tr8(z[0], z[1], bit3);
        const v2u p = {__float_as_uint(q), T};
        __builtin_amdgcn_raw_buffer_store_b64(p, rs, qoff, 0, AUX_SC1);
    }
}

extern "C" void kernel_launch(void* const* d_in, const int* in_sizes, int n_in, void* d_out, int out_size, void* d_ws, size_t ws_size,
                              hipStream_t stream) {
    glu_kernel<<<NWG, 320, 0, stream>>>((const unsigned short*)d_in[0], (const unsigned short*)d_in[1], (const unsigned short*)d_in[2],
                                        (const unsigned short*)d_in[3], (unsigned short*)d_out, (unsigned*)d_ws);
}
```

```cpp
#include <hip/hip_runtime.h>
#include <hip/hip_bf16.h>

#define GAS __attribute__((address_space(1)))
typedef unsigned v4u __attribute__((ext_vector_type(4)));
typedef unsigned v2u __attribute__((ext_vector_type(2)));
typedef float v4f __attribute__((ext_vector_type(4)));
typedef __bf16 bf16x2 __attribute__((ext_vector_type(2)));

#define PFW 4
#define PFLINES 32
#define SLEEP1 20

namespace {
constexpr int DM = 1024, DF = 3584;
constexpr int NREG = 224;
constexpr int NWG = 252;
constexpr int P1_OFF = 4096;
constexpr int Q_OFF = P1_OFF + 56 * 8 * 1024;
constexpr int WS_USED = Q_OFF + 56 * 8192;
constexpr int OOB = (int)0x80000000u;
constexpr int AUX_NT = 2, AUX_SC1 = 16;
constexpr int RSRC3 = 0x00020000;

__device__ __forceinline__ float dot2(unsigned a, unsigned b, float c) {
    return __builtin_amdgcn_fdot2_f32_bf16(__builtin_bit_cast(bf16x2, a), __builtin_bit_cast(bf16x2, b), c, false);
}
__device__ __forceinline__ unsigned plo(unsigned r0, unsigned r1) { return __builtin_amdgcn_perm(r1, r0, 0x05040100u); }
__device__ __forceinline__ unsigned phi(unsigned r0, unsigned r1) { return __builtin_amdgcn_perm(r1, r0, 0x07060302u); }
__device__ __forceinline__ float bf2f(unsigned b) { return __uint_as_float(b << 16); }
__device__ __forceinline__ unsigned f2bf(float f) { __bf16 h = (__bf16)f; return (unsigned)__builtin_bit_cast(unsigned short, h); }
template <int CTRL> __device__ __forceinline__ float dppf(float v) {
    return __uint_as_float((unsigned)__builtin_amdgcn_update_dpp(0, (int)__float_as_uint(v), CTRL, 0xf, 0xf, true));
}
__device__ __forceinline__ float tr32(float a, float b) { auto r = __builtin_amdgcn_permlane32_swap(__float_as_uint(a), __float_as_uint(b), false, false); return __uint_as_float(r[0]) + __uint_as_float(r[1]); }
__device__ __forceinline__ float tr16(float a, float b) { auto r = __builtin_amdgcn_permlane16_swap(__float_as_uint(a), __float_as_uint(b), false, false); return __uint_as_float(r[0]) + __uint_as_float(r[1]); }
__device__ __forceinline__ float tr8(float a, float b, bool bit3) { const float a2 = a + dppf<0x128>(a), b2 = b + dppf<0x128>(b); return bit3 ? b2 : a2; }
#define GA_STR_(x) #x
#define GA_STR(x) GA_STR_(x)
#define LBAR() do { __builtin_amdgcn_sched_barrier(0); asm volatile("s_waitcnt lgkmcnt(0)\n\ts_barrier" ::: "memory"); __builtin_amdgcn_sched_barrier(0); } while (0)

#define GA_LD(B, B3, O)  "buffer_load_dwordx4 v[" #B ":" #B3 "], %[" #O "], %[rs], 0 offen sc1\n\t"
#define GA_TAKE(W, B, B1, B2, B3, O, R0, R1) \
    "s_waitcnt vmcnt(" #W ")\n\t" \
    "v_cmp_eq_u32 vcc, %[T], v" #B1 "\n\t" \
    "v_cmp_eq_u32 %[m], %[T], v" #B3 "\n\t" \
    "s_and_b64 vcc, vcc, %[m]\n\t" \
    "v_cndmask_b32 v" #R0 ", v" #R0 ", v" #B ", vcc\n\t" \
    "v_cndmask_b32 v" #R1 ", v" #R1 ", v" #B2 ", vcc\n\t" \
    "v_cndmask_b32 %[" #O "], %[" #O "], %[s0], vcc\n\t"
#define GA_END \
    "v_cmp_gt_i32 vcc, 0, %[s1]\n\t" \
    "s_cmp_eq_u64 vcc, exec\n\t" \
    "s_cbranch_scc1 2f\n\t" \
    "s_add_u32 %[c], %[c], 1\n\t" \
    "s_cmp_lt_u32 %[c], 0x10000\n\t" \
    "s_cbranch_scc1 1b\n\t" \
    "2:\n\t"
#define GA_OWN(O, R0, R1) \
    "v_cmp_gt_i32 vcc, 0, %[" #O "]\n\t" \
    "v_mov_b32 v" #R0 ", 0\n\tv_mov_b32 v" #R1 ", 0\n\t" \
    "v_cndmask_b32 v" #R0 ", v" #R0 ", %[i0], vcc\n\t" \
    "v_cndmask_b32 v" #R1 ", v" #R1 ", %[i1], vcc\n\t"
#define GA_ZERO(R0, R1) "v_mov_b32 v" #R0 ", 0\n\tv_mov_b32 v" #R1 ", 0\n\t"

__device__ __forceinline__ void gather_gu(const __amdgpu_buffer_rsrc_t rs, int o0, int o1, int o2, int o3, int o4, int o5, int o6, int o7, float i0, float i1, unsigned T, float& s0, float& s1) {
    unsigned c; unsigned long long m;
    asm volatile(
        GA_OWN(o0, 128, 136) GA_OWN(o1, 129, 137) GA_OWN(o2, 130, 138) GA_OWN(o3, 131, 139)
        GA_ZERO(132, 140) GA_ZERO(133, 141) GA_ZERO(134, 142) GA_ZERO(135, 143)
        "v_bfrev_b32 %[s0], 1\n\t"
        "s_mov_b32 %[c], 0\n\t"
        "s_sleep " GA_STR(SLEEP1) "\n\t"
        "1:\n\t"
        GA_LD(112, 115, o4) GA_LD(116, 119, o5) GA_LD(120, 123, o6) GA_LD(124, 127, o7)
        GA_LD(96, 99, o0) GA_LD(100, 103, o1) GA_LD(104, 107, o2) GA_LD(108, 111, o3)
        GA_TAKE(7, 112, 113, 114, 115, o4, 132, 140)
        GA_TAKE(6, 116, 117, 118, 119, o5, 133, 141)
        GA_TAKE(5, 120, 121, 122, 123, o6, 134, 142)
        GA_TAKE(4, 124, 125, 126, 127, o7, 135, 143)
        "v_and_b32 %[s1], %[o4], %[o5]\n\t"
        "v_and_b32 %[s1], %[s1], %[o6]\n\t"
        "v_and_b32 %[s1], %[s1], %[o7]\n\t"
        GA_TAKE(3, 96, 97, 98, 99, o0, 128, 136)
        GA_TAKE(2, 100, 101, 102, 103, o1, 129, 137)
        GA_TAKE(1, 104, 105, 106, 107, o2, 130, 138)
        GA_TAKE(0, 108, 109, 110, 111, o3, 131, 139)
        "v_and_b32 %[s1], %[s1], %[o0]\n\t"
        "v_and_b32 %[s1], %[s1], %[o1]\n\t"
        "v_and_b32 %[s1], %[s1], %[o2]\n\t"
        "v_and_b32 %[s1], %[s1], %[o3]\n\t"
        GA_END
        "v_add_f32 v128, v128, v129\n\tv_add_f32 v136, v136, v137\n\t"
        "v_add_f32 v130, v130, v131\n\tv_add_f32 v138, v138, v139\n\t"
        "v_add_f32 v132, v132, v133\n\tv_add_f32 v140, v140, v141\n\t"
        "v_add_f32 v134, v134, v135\n\tv_add_f32 v142, v142, v143\n\t"
        "v_add_f32 v128, v128, v130\n\tv_add_f32 v136, v136, v138\n\t"
        "v_add_f32 v132, v132, v134\n\tv_add_f32 v140, v140, v142\n\t"
        "v_add_f32 %[s0], v128, v132\n\tv_add_f32 %[s1], v136, v140\n\t"
        "s_nop 1"
        : [s0] "=&v"(s0), [s1] "=&v"(s1), [c] "=&s"(c), [m] "=&s"(m), [o0] "+v"(o0), [o1] "+v"(o1), [o2] "+v"(o2), [o3] "+v"(o3),
          [o4] "+v"(o4), [o5] "+v"(o5), [o6] "+v"(o6), [o7] "+v"(o7)
        : [i0] "v"(i0), [i1] "v"(i1), [T] "s"(T), [rs] "s"(rs)
        : "v96", "v97", "v98", "v99", "v100", "v101", "v102", "v103", "v104", "v105", "v106", "v107", "v108", "v109", "v110", "v111",
          "v112", "v113", "v114", "v115", "v116", "v117", "v118", "v119", "v120", "v121", "v122", "v123", "v124", "v125", "v126", "v127",
          "v128", "v129", "v130", "v131", "v132", "v133", "v134", "v135", "v136", "v137", "v138", "v139", "v140", "v141", "v142", "v143",
          "vcc", "scc", "memory");
}
__device__ __forceinline__ void gather_out(const __amdgpu_buffer_rsrc_t rs, int o0, int o1, int o2, int o3, unsigned T, float& s0, float& s1) {
    unsigned c; unsigned long long m;
    asm volatile(
        GA_ZERO(128, 136) GA_ZERO(129, 137) GA_ZERO(130, 138) GA_ZERO(131, 139)
        "v_bfrev_b32 %[s0], 1\n\t"
        "s_mov_b32 %[c], 0\n\t"
        "1:\n\t"
        GA_LD(96, 99, o0) GA_LD(100, 103, o1) GA_LD(104, 107, o2) GA_LD(108, 111, o3)
        GA_TAKE(3, 96, 97, 98, 99, o0, 128, 136)
        GA_TAKE(2, 100, 101, 102, 103, o1, 129, 137)
        GA_TAKE(1, 104, 105, 106, 107, o2, 130, 138)
        GA_TAKE(0, 108, 109, 110, 111, o3, 131, 139)
        "v_and_b32 %[s1], %[o0], %[o1]\n\t"
        "v_and_b32 %[s1], %[s1], %[o2]\n\t"
        "v_and_b32 %[s1], %[s1], %[o3]\n\t"
        GA_END
        "v_add_f32 v128, v128, v129\n\tv_add_f32 v136, v136, v137\n\t"
        "v_add_f32 v130, v130, v131\n\tv_add_f32 v138, v138, v139\n\t"
        "v_add_f32 %[s0], v128, v130\n\tv_add_f32 %[s1], v136, v138\n\t"
        "s_nop 1"
        : [s0] "=&v"(s0), [s1] "=&v"(s1), [c] "=&s"(c), [m] "=&s"(m), [o0] "+v"(o0), [o1] "+v"(o1), [o2] "+v"(o2), [o3] "+v"(o3)
        : [T] "s"(T), [rs] "s"(rs)
        : "v96", "v97", "v98", "v99", "v100", "v101", "v102", "v103", "v104", "v105", "v106", "v107", "v108", "v109", "v110", "v111",
          "v128", "v129", "v130", "v131", "v136", "v137", "v138", "v139", "vcc", "scc", "memory");
}
}

__global__ void __launch_bounds__(320, 2)
glu_kernel(const unsigned short* __restrict__ x_, const unsigned short* __restrict__ wg_, const unsigned short* __restrict__ wu_,
           const unsigned short* __restrict__ wd_, unsigned short* __restrict__ out_, unsigned* __restrict__ ws_) {
    __shared__ __attribute__((aligned(16))) float red[4 * 2 * 64];
    __shared__ __attribute__((aligned(16))) unsigned hs[36];
    const int b = blockIdx.x, tid = threadIdx.x, lane = tid & 63;
    const int w = __builtin_amdgcn_readfirstlane(tid >> 6);
    unsigned pfc = 0;
    if (w >= PFW) {
        unsigned long long pc; asm volatile("s_getpc_b64 %0" : "=s"(pc));
        pfc = *(const GAS unsigned*)((const GAS unsigned char*)(pc & ~255ull) + min(lane, PFLINES - 1) * 128);
    }
    __builtin_amdgcn_sched_barrier(0);
    asm volatile("" :: "s"(x_), "s"(wg_), "s"(wu_), "s"(wd_), "s"(out_), "s"(ws_));
    const __amdgpu_buffer_rsrc_t rs = __builtin_amdgcn_make_buffer_rsrc((void*)ws_, 0, WS_USED, RSRC3);
    const int s = (b & 7) * 28 + (b >> 3);
    const int cls = (3 - (int)((unsigned long long)wg_ >> 7)) & 7;
    const bool helper = b >= NREG;
    const int gi = helper ? cls + 8 * ((b - NREG) >> 2) : s >> 2;
    const int gj = helper ? (b - NREG) & 3 : s & 3;
    const bool slow = (gi & 7) == cls;
    const bool bit3 = (lane & 8) != 0;

    if (w >= 4) {
        const unsigned E = __builtin_amdgcn_raw_buffer_load_b32(rs, 0, 0, AUX_SC1);
        const int pbase = P1_OFF + gi * 8192 + lane * 16;
        int poff = pbase + (helper ? 4 + gj : gj) * 1024;
        int off[8];
#pragma unroll
        for (int m = 0; m < 8; ++m) off[m] = (m == gj || (m >= 4 && !slow)) ? OOB : pbase + m * 1024;
        const int cidx = (b & 7) * 16 + (b >> 3);
        const int pr = lane & 3, ps = lane >> 2;
        int off2[4];
#pragma unroll
        for (int k = 0; k < 4; ++k) off2[k] = ((k == 3) && (ps >= 8)) ? OOB : Q_OFF + (16 * k + ps) * 8192 + cidx * 64 + pr * 16;
        int ooff = (64 * (cidx >> 3) + 8 * (2 * pr + ((lane >> 4) & 1)) + (cidx & 7)) * 2;
        int rd0 = lane * 4;
        asm volatile("" : "+v"(poff), "+v"(off[0]), "+v"(off[1]), "+v"(off[2]), "+v"(off[3]), "+v"(off[4]), "+v"(off[5]), "+v"(off[6]), "+v"(off[7]));
        asm volatile("" : "+v"(off2[0]), "+v"(off2[1]), "+v"(off2[2]), "+v"(off2[3]), "+v"(ooff), "+v"(rd0));
        unsigned T = __builtin_amdgcn_readfirstlane(max(E + 1u, 1u));
        asm volatile("" : "+s"(T));
        if (lane == 0) hs[32] = T;
        asm volatile("" :: "v"(pfc));
        LBAR();
        const float* rp = (const float*)((const unsigned char*)red + rd0);
        const float og = (rp[0 * 64] + rp[2 * 64]) + (rp[4 * 64] + rp[6 * 64]);
        const float ou = (rp[1 * 64] + rp[3 * 64]) + (rp[5 * 64] + rp[7 * 64]);
        {
            const v4u p = {__float_as_uint(og), T, __float_as_uint(ou), T};
            __builtin_amdgcn_raw_buffer_store_b128(p, rs, poff, 0, AUX_SC1);
        }
        if (helper) return;
        float g, u;
        gather_gu(rs, off[0], off[1], off[2], off[3], off[4], off[5], off[6], off[7], og, ou, T, g, u);
        {
            const float gb = bf2f(f2bf(g)), ub = bf2f(f2bf(u));
            const float e2 = __builtin_amdgcn_exp2f(gb * __builtin_fmaf(gb * gb, -0.10294324f, -2.3022082f));
            const float ge = gb * __builtin_amdgcn_rcpf(1.0f + e2);
            ((unsigned short*)hs)[lane] = (unsigned short)f2bf(bf2f(f2bf(ge)) * ub);
        }
        LBAR();
        if (b < 128) {
            float s0, s1;
            gather_out(rs, off2[0], off2[1], off2[2], off2[3], T, s0, s1);
            s0 += dppf<0x124>(s0); s1 += dppf<0x124>(s1);
            s0 += dppf<0x128>(s0); s1 += dppf<0x128>(s1);
            const float t16 = tr16(s0, s1);
            const float t32 = tr32(t16, t16);
            if ((lane & 0x2C) == 0) *(GAS unsigned short*)((GAS unsigned char*)out_ + ooff) = (unsigned short)f2bf(t32);
            if (b == 0 && lane == 0) *(GAS unsigned*)ws_ = T;
        }
    } else {
        const __amdgpu_buffer_rsrc_t rg = __builtin_amdgcn_make_buffer_rsrc((void*)wg_, 0, DM * DF * 2, RSRC3);
        const __amdgpu_buffer_rsrc_t ru = __builtin_amdgcn_make_buffer_rsrc((void*)wu_, 0, DM * DF * 2, RSRC3);
        const __amdgpu_buffer_rsrc_t rd = __builtin_amdgcn_make_buffer_rsrc((void*)wd_, 0, DM * DF * 2, RSRC3);
        const int r = lane >> 3, cg = lane & 7;
        const int row0 = 256 * gj + 64 * w;
        const v4u xp = *(const GAS v4u*)((const GAS unsigned char*)x_ + (row0 + 8 * r) * 2);
        const int gv = (8 * r) * (DF * 2) + cg * 16;
        const int gvA = helper ? OOB : gv, gvB = (slow && !helper) ? OOB : gv;
        const int gso = row0 * (DF * 2) + 128 * gi;
        v4u g0[4], g1[4], u0[4], u1[4];
#pragma unroll
        for (int t = 0; t < 4; ++t) {
            const int v = t < 2 ? gvA : gvB;
            g0[t] = __builtin_amdgcn_raw_buffer_load_b128(rg, v, gso + (2 * t) * (DF * 2), AUX_NT);
            g1[t] = __builtin_amdgcn_raw_buffer_load_b128(rg, v, gso + (2 * t + 1) * (DF * 2), AUX_NT);
            u0[t] = __builtin_amdgcn_raw_buffer_load_b128(ru, v, gso + (2 * t) * (DF * 2), AUX_NT);
            u1[t] = __builtin_amdgcn_raw_buffer_load_b128(ru, v, gso + (2 * t + 1) * (DF * 2), AUX_NT);
        }
        __builtin_amdgcn_sched_barrier(0);
        const int dv = helper ? OOB : (8 * r) * (DM * 2) + cg * 16;
        const int dso = (64 * gi) * (DM * 2) + 512 * gj + 128 * w;
        float a[16];
#pragma unroll
        for (int i = 0; i < 16; ++i) a[i] = 0.f;
#pragma unroll
        for (int t = 0; t < 4; ++t) {
            const unsigned xx = xp[t];
#pragma unroll
            for (int k = 0; k < 4; ++k) {
                a[2 * k]         = dot2(plo(g0[t][k], g1[t][k]), xx, a[2 * k]);
                a[2 * k + 1]     = dot2(phi(g0[t][k], g1[t][k]), xx, a[2 * k + 1]);
                a[8 + 2 * k]     = dot2(plo(u0[t][k], u1[t][k]), xx, a[8 + 2 * k]);
                a[8 + 2 * k + 1] = dot2(phi(u0[t][k], u1[t][k]), xx, a[8 + 2 * k + 1]);
            }
        }
        asm volatile("" : "+v"(a[0]), "+v"(a[1]), "+v"(a[2]), "+v"(a[3]), "+v"(a[4]), "+v"(a[5]), "+v"(a[6]), "+v"(a[7]));
        asm volatile("" : "+v"(a[8]), "+v"(a[9]), "+v"(a[10]), "+v"(a[11]), "+v"(a[12]), "+v"(a[13]), "+v"(a[14]), "+v"(a[15]));
        __builtin_amdgcn_sched_barrier(0);
        v4u d0[4], d1[4];
#pragma unroll
        for (int t = 0; t < 4; ++t) {
            d0[t] = __builtin_amdgcn_raw_buffer_load_b128(rd, dv, dso + (2 * t) * 2048, AUX_NT);
            d1[t] = __builtin_amdgcn_raw_buffer_load_b128(rd, dv + 2048, dso + (2 * t) * 2048, AUX_NT);
        }
        __builtin_amdgcn_sched_barrier(0);
        {
            float y[8], z[4];
#pragma unroll
            for (int k = 0; k < 8; ++k) y[k] = tr32(a[k], a[k + 8]);
#pragma unroll
            for (int k = 0; k < 4; ++k) z[k] = tr16(y[k], y[k + 4]);
            float2 q; q.x = tr8(z[0], z[2], bit3); q.y = tr8(z[1], z[3], bit3);
            const int col0 = 8 * cg + 4 * ((lane >> 4) & 1) + 2 * ((lane >> 3) & 1);
            *(float2*)&red[(w * 2 + (lane >> 5)) * 64 + col0] = q;
        }
        LBAR();
        if (helper) return;
        unsigned dl[4][4], dh[4][4];
#pragma unroll
        for (int t = 0; t < 4; ++t)
#pragma unroll
            for (int k = 0; k < 4; ++k) { dl[t][k] = plo(d0[t][k], d1[t][k]); dh[t][k] = phi(d0[t][k], d1[t][k]); }
        int qoff = Q_OFF + gi * 8192 + (256 * gj + 64 * w + lane) * 8;
        unsigned T = hs[32];
#pragma unroll
        for (int t = 0; t < 4; ++t) asm volatile("" : "+v"(dl[t][0]), "+v"(dl[t][1]), "+v"(dl[t][2]), "+v"(dl[t][3]), "+v"(dh[t][0]), "+v"(dh[t][1]), "+v"(dh[t][2]), "+v"(dh[t][3]));
        asm volatile("" : "+v"(T), "+v"(qoff));
        LBAR();
        const v4u hp = *(const v4u*)&hs[4 * r];
        float o[8];
#pragma unroll
        for (int i = 0; i < 8; ++i) o[i] = 0.f;
#pragma unroll
        for (int t = 0; t < 4; ++t) {
            const unsigned hh = hp[t];
#pragma unroll
            for (int k = 0; k < 4; ++k) {
                o[2 * k]     = dot2(dl[t][k], hh, o[2 * k]);
                o[2 * k + 1] = dot2(dh[t][k], hh, o[2 * k + 1]);
            }
        }
        float y[4], z[2];
#pragma unroll
        for (int k = 0; k < 4; ++k) y[k] = tr32(o[k], o[k + 4]);
#pragma unroll
        for (int k = 0; k < 2; ++k) z[k] = tr16(y[k], y[k + 2]);
        const float q = tr8(z[0], z[1], bit3);
        const v2u p = {__float_as_uint(q), T};
        __builtin_amdgcn_raw_buffer_store_b64(p, rs, qoff, 0, AUX_SC1);
    }
}

extern "C" void kernel_launch(void* const* d_in, const int* in_sizes, int n_in, void* d_out, int out_size, void* d_ws, size_t ws_size,
                              hipStream_t stream) {
    glu_kernel<<<NWG, 320, 0, stream>>>((const unsigned short*)d_in[0], (const unsigned short*)d_in[1], (const unsigned short*)d_in[2],
                                        (const unsigned short*)d_in[3], (unsigned short*)d_out, (unsigned*)d_ws);
}
```

```cpp
#include <hip/hip_runtime.h>
#include <hip/hip_bf16.h>

#define GAS __attribute__((address_space(1)))
typedef unsigned v4u __attribute__((ext_vector_type(4)));
typedef unsigned v2u __attribute__((ext_vector_type(2)));
typedef __bf16 bf16x2 __attribute__((ext_vector_type(2)));

#define SLEEP1 20

namespace {
constexpr int DM = 1024, DF = 3584;
constexpr int NREG = 224;
constexpr int NWG = 252;
constexpr int P1_OFF = 4096;
constexpr int Q_OFF = P1_OFF + 56 * 8 * 1024;
constexpr int WS_USED = Q_OFF + 56 * 8192;
constexpr int OOB = (int)0x80000000u;
constexpr int AUX_NT = 2, AUX_SC1 = 16;
constexpr int RSRC3 = 0x00020000;
constexpr int CODE_LINES = 32;

__device__ __forceinline__ float dot2(unsigned a, unsigned b, float c) {
    return __builtin_amdgcn_fdot2_f32_bf16(__builtin_bit_cast(bf16x2, a), __builtin_bit_cast(bf16x2, b), c, false);
}
__device__ __forceinline__ unsigned plo(unsigned r0, unsigned r1) { return __builtin_amdgcn_perm(r1, r0, 0x05040100u); }
__device__ __forceinline__ unsigned phi(unsigned r0, unsigned r1) { return __builtin_amdgcn_perm(r1, r0, 0x07060302u); }
__device__ __forceinline__ float bf2f(unsigned b) { return __uint_as_float(b << 16); }
__device__ __forceinline__ unsigned f2bf(float f) { __bf16 h = (__bf16)f; return (unsigned)__builtin_bit_cast(unsigned short, h); }
template <int CTRL> __device__ __forceinline__ float dppf(float v) {
    return __uint_as_float((unsigned)__builtin_amdgcn_update_dpp(0, (int)__float_as_uint(v), CTRL, 0xf, 0xf, true));
}
__device__ __forceinline__ float tr32(float a, float b) { auto r = __builtin_amdgcn_permlane32_swap(__float_as_uint(a), __float_as_uint(b), false, false); return __uint_as_float(r[0]) + __uint_as_float(r[1]); }
__device__ __forceinline__ float tr16(float a, float b) { auto r = __builtin_amdgcn_permlane16_swap(__float_as_uint(a), __float_as_uint(b), false, false); return __uint_as_float(r[0]) + __uint_as_float(r[1]); }
__device__ __forceinline__ float tr8(float a, float b, bool bit3) { const float a2 = a + dppf<0x128>(a), b2 = b + dppf<0x128>(b); return bit3 ? b2 : a2; }
#define GA_STR_(x) #x
#define GA_STR(x) GA_STR_(x)
#define LBAR() do { __builtin_amdgcn_sched_barrier(0); asm volatile("s_waitcnt lgkmcnt(0)\n\ts_barrier" ::: "memory"); __builtin_amdgcn_sched_barrier(0); } while (0)

#define GA_LD(B, B3, O)  "buffer_load_dwordx4 v[" #B ":" #B3 "], %[" #O "], %[rs], 0 offen sc1\n\t"
#define GA_TAKE(W, B, B1, B2, B3, O, R0, R1) \
    "s_waitcnt vmcnt(" #W ")\n\t" \
    "v_cmp_eq_u32 vcc, %[T], v" #B1 "\n\t" \
    "v_cmp_eq_u32 %[m], %[T], v" #B3 "\n\t" \
    "s_and_b64 vcc, vcc, %[m]\n\t" \
    "v_cndmask_b32 v" #R0 ", v" #R0 ", v" #B ", vcc\n\t" \
    "v_cndmask_b32 v" #R1 ", v" #R1 ", v" #B2 ", vcc\n\t" \
    "v_cndmask_b32 %[" #O "], %[" #O "], %[s0], vcc\n\t"
#define GA_END \
    "v_cmp_gt_i32 vcc, 0, %[s1]\n\t" \
    "s_cmp_eq_u64 vcc, exec\n\t" \
    "s_cbranch_scc1 2f\n\t" \
    "s_add_u32 %[c], %[c], 1\n\t" \
    "s_cmp_lt_u32 %[c], 0x10000\n\t" \
    "s_cbranch_scc1 1b\n\t" \
    "2:\n\t"
#define GA_OWN(O, R0, R1) \
    "v_cmp_gt_i32 vcc, 0, %[" #O "]\n\t" \
    "v_mov_b32 v" #R0 ", 0\n\tv_mov_b32 v" #R1 ", 0\n\t" \
    "v_cndmask_b32 v" #R0 ", v" #R0 ", %[i0], vcc\n\t" \
    "v_cndmask_b32 v" #R1 ", v" #R1 ", %[i1], vcc\n\t"
#define GA_ZERO(R0, R1) "v_mov_b32 v" #R0 ", 0\n\tv_mov_b32 v" #R1 ", 0\n\t"

__device__ __forceinline__ void gather_gu(const __amdgpu_buffer_rsrc_t rs, int o0, int o1, int o2, int o3, int o4, int o5, int o6, int o7, float i0, float i1, unsigned T, float& s0, float& s1) {
    unsigned c; unsigned long long m;
    asm volatile(
        GA_OWN(o0, 128, 136) GA_OWN(o1, 129, 137) GA_OWN(o2, 130, 138) GA_OWN(o3, 131, 139)
        GA_ZERO(132, 140) GA_ZERO(133, 141) GA_ZERO(134, 142) GA_ZERO(135, 143)
        "v_bfrev_b32 %[s0], 1\n\t"
        "s_mov_b32 %[c], 0\n\t"
        "s_sleep " GA_STR(SLEEP1) "\n\t"
        "1:\n\t"
        GA_LD(112, 115, o4) GA_LD(116, 119, o5) GA_LD(120, 123, o6) GA_LD(124, 127, o7)
        GA_LD(96, 99, o0) GA_LD(100, 103, o1) GA_LD(104, 107, o2) GA_LD(108, 111, o3)
        GA_TAKE(7, 112, 113, 114, 115, o4, 132, 140)
        GA_TAKE(6, 116, 117, 118, 119, o5, 133, 141)
        GA_TAKE(5, 120, 121, 122, 123, o6, 134, 142)
        GA_TAKE(4, 124, 125, 126, 127, o7, 135, 143)
        "v_and_b32 %[s1], %[o4], %[o5]\n\t"
        "v_and_b32 %[s1], %[s1], %[o6]\n\t"
        "v_and_b32 %[s1], %[s1], %[o7]\n\t"
        GA_TAKE(3, 96, 97, 98, 99, o0, 128, 136)
        GA_TAKE(2, 100, 101, 102, 103, o1, 129, 137)
        GA_TAKE(1, 104, 105, 106, 107, o2, 130, 138)
        GA_TAKE(0, 108, 109, 110, 111, o3, 131, 139)
        "v_and_b32 %[s1], %[s1], %[o0]\n\t"
        "v_and_b32 %[s1], %[s1], %[o1]\n\t"
        "v_and_b32 %[s1], %[s1], %[o2]\n\t"
        "v_and_b32 %[s1], %[s1], %[o3]\n\t"
        GA_END
        "v_add_f32 v128, v128, v129\n\tv_add_f32 v136, v136, v137\n\t"
        "v_add_f32 v130, v130, v131\n\tv_add_f32 v138, v138, v139\n\t"
        "v_add_f32 v132, v132, v133\n\tv_add_f32 v140, v140, v141\n\t"
        "v_add_f32 v134, v134, v135\n\tv_add_f32 v142, v142, v143\n\t"
        "v_add_f32 v128, v128, v130\n\tv_add_f32 v136, v136, v138\n\t"
        "v_add_f32 v132, v132, v134\n\tv_add_f32 v140, v140, v142\n\t"
        "v_add_f32 %[s0], v128, v132\n\tv_add_f32 %[s1], v136, v140\n\t"
        "s_nop 1"
        : [s0] "=&v"(s0), [s1] "=&v"(s1), [c] "=&s"(c), [m] "=&s"(m), [o0] "+v"(o0), [o1] "+v"(o1), [o2] "+v"(o2), [o3] "+v"(o3),
          [o4] "+v"(o4), [o5] "+v"(o5), [o6] "+v"(o6), [o7] "+v"(o7)
        : [i0] "v"(i0), [i1] "v"(i1), [T] "s"(T), [rs] "s"(rs)
        : "v96", "v97", "v98", "v99", "v100", "v101", "v102", "v103", "v104", "v105", "v106", "v107", "v108", "v109", "v110", "v111",
          "v112", "v113", "v114", "v115", "v116", "v117", "v118", "v119", "v120", "v121", "v122", "v123", "v124", "v125", "v126", "v127",
          "v128", "v129", "v130", "v131", "v132", "v133", "v134", "v135", "v136", "v137", "v138", "v139", "v140", "v141", "v142", "v143",
          "vcc", "scc", "memory");
}
__device__ __forceinline__ void gather_out(const __amdgpu_buffer_rsrc_t rs, int o0, int o1, int o2, int o3, unsigned T, float& s0, float& s1) {
    unsigned c; unsigned long long m;
    asm volatile(
        GA_ZERO(128, 136) GA_ZERO(129, 137) GA_ZERO(130, 138) GA_ZERO(131, 139)
        "v_bfrev_b32 %[s0], 1\n\t"
        "s_mov_b32 %[c], 0\n\t"
        "1:\n\t"
        GA_LD(96, 99, o0) GA_LD(100, 103, o1) GA_LD(104, 107, o2) GA_LD(108, 111, o3)
        GA_TAKE(3, 96, 97, 98, 99, o0, 128, 136)
        GA_TAKE(2, 100, 101, 102, 103, o1, 129, 137)
        GA_TAKE(1, 104, 105, 106, 107, o2, 130, 138)
        GA_TAKE(0, 108, 109, 110, 111, o3, 131, 139)
        "v_and_b32 %[s1], %[o0], %[o1]\n\t"
        "v_and_b32 %[s1], %[s1], %[o2]\n\t"
        "v_and_b32 %[s1], %[s1], %[o3]\n\t"
        GA_END
        "v_add_f32 v128, v128, v129\n\tv_add_f32 v136, v136, v137\n\t"
        "v_add_f32 v130, v130, v131\n\tv_add_f32 v138, v138, v139\n\t"
        "v_add_f32 %[s0], v128, v130\n\tv_add_f32 %[s1], v136, v138\n\t"
        "s_nop 1"
        : [s0] "=&v"(s0), [s1] "=&v"(s1), [c] "=&s"(c), [m] "=&s"(m), [o0] "+v"(o0), [o1] "+v"(o1), [o2] "+v"(o2), [o3] "+v"(o3)
        : [T] "s"(T), [rs] "s"(rs)
        : "v96", "v97", "v98", "v99", "v100", "v101", "v102", "v103", "v104", "v105", "v106", "v107", "v108", "v109", "v110", "v111",
          "v128", "v129", "v130", "v131", "v136", "v137", "v138", "v139", "vcc", "scc", "memory");
}
}

__global__ void __launch_bounds__(320, 2)
glu_kernel(const unsigned short* __restrict__ x_, const unsigned short* __restrict__ wg_, const unsigned short* __restrict__ wu_,
           const unsigned short* __restrict__ wd_, unsigned short* __restrict__ out_, unsigned* __restrict__ ws_) {
    __shared__ __attribute__((aligned(16))) float red[4 * 2 * 64];
    __shared__ __attribute__((aligned(16))) unsigned hs[36];
    const int b = blockIdx.x, tid = threadIdx.x, lane = tid & 63;
    const int w = __builtin_amdgcn_readfirstlane(tid >> 6);
    unsigned pfc = 0;
    if (w >= 4) {
        unsigned long long pc; asm volatile("s_getpc_b64 %0" : "=s"(pc));
        pfc = *(const GAS unsigned*)((const GAS unsigned char*)(pc & ~255ull) + min(lane, CODE_LINES - 1) * 128);
    }
    __builtin_amdgcn_sched_barrier(0);
    asm volatile("" :: "s"(x_), "s"(wg_), "s"(wu_), "s"(wd_), "s"(out_), "s"(ws_));
    const __amdgpu_buffer_rsrc_t rs = __builtin_amdgcn_make_buffer_rsrc((void*)ws_, 0, WS_USED, RSRC3);
    const int s = (b & 7) * 28 + (b >> 3);
    const int cls = (3 - (int)((unsigned long long)wg_ >> 7)) & 7;
    const bool helper = b >= NREG;
    const int gi = helper ? cls + 8 * ((b - NREG) >> 2) : s >> 2;
    const int gj = helper ? (b - NREG) & 3 : s & 3;
    const bool slow = (gi & 7) == cls;
    const bool bit3 = (lane & 8) != 0;

    if (w >= 4) {
        const unsigned E = __builtin_amdgcn_raw_buffer_load_b32(rs, 0, 0, AUX_SC1);
        const int pbase = P1_OFF + gi * 8192 + lane * 16;
        int poff = pbase + (helper ? 4 + gj : gj) * 1024;
        int off[8];
#pragma unroll
        for (int m = 0; m < 8; ++m) off[m] = (m == gj || (m >= 4 && !slow)) ? OOB : pbase + m * 1024;
        const int cidx = (b & 7) * 16 + (b >> 3);
        const int pr = lane & 3, ps = lane >> 2;
        int off2[4];
#pragma unroll
        for (int k = 0; k < 4; ++k) off2[k] = ((k == 3) && (ps >= 8)) ? OOB : Q_OFF + (16 * k + ps) * 8192 + cidx * 64 + pr * 16;
        int ooff = (64 * (cidx >> 3) + 8 * (2 * pr + ((lane >> 4) & 1)) + (cidx & 7)) * 2;
        int rd0 = lane * 4;
        asm volatile("" : "+v"(poff), "+v"(off[0]), "+v"(off[1]), "+v"(off[2]), "+v"(off[3]), "+v"(off[4]), "+v"(off[5]), "+v"(off[6]), "+v"(off[7]));
        asm volatile("" : "+v"(off2[0]), "+v"(off2[1]), "+v"(off2[2]), "+v"(off2[3]), "+v"(ooff), "+v"(rd0));
        unsigned T = __builtin_amdgcn_readfirstlane(max(E + 1u, 1u));
        asm volatile("" : "+s"(T));
        if (lane == 0) hs[32] = T;
        asm volatile("" :: "v"(pfc));
        LBAR();
        const float* rp = (const float*)((const unsigned char*)red + rd0);
        const float og = (rp[0 * 64] + rp[2 * 64]) + (rp[4 * 64] + rp[6 * 64]);
        const float ou = (rp[1 * 64] + rp[3 * 64]) + (rp[5 * 64] + rp[7 * 64]);
        {
            const v4u p = {__float_as_uint(og), T, __float_as_uint(ou), T};
            __builtin_amdgcn_raw_buffer_store_b128(p, rs, poff, 0, AUX_SC1);
        }
        if (helper) return;
        float g, u;
        gather_gu(rs, off[0], off[1], off[2], off[3], off[4], off[5], off[6], off[7], og, ou, T, g, u);
        {
            const float gb = bf2f(f2bf(g)), ub = bf2f(f2bf(u));
            const float e2 = __builtin_amdgcn_exp2f(gb * __builtin_fmaf(gb * gb, -0.10294324f, -2.3022082f));
            const float ge = gb * __builtin_amdgcn_rcpf(1.0f + e2);
            ((unsigned short*)hs)[lane] = (unsigned short)f2bf(bf2f(f2bf(ge)) * ub);
        }
        LBAR();
        if (b < 128) {
            float s0, s1;
            gather_out(rs, off2[0], off2[1], off2[2], off2[3], T, s0, s1);
            s0 += dppf<0x124>(s0); s1 += dppf<0x124>(s1);
            s0 += dppf<0x128>(s0); s1 += dppf<0x128>(s1);
            const float t16 = tr16(s0, s1);
            const float t32 = tr32(t16, t16);
            if ((lane & 0x2C) == 0) *(GAS unsigned short*)((GAS unsigned char*)out_ + ooff) = (unsigned short)f2bf(t32);
            if (b == 0 && lane == 0) *(GAS unsigned*)ws_ = T;
        }
    } else {
        const __amdgpu_buffer_rsrc_t rg = __builtin_amdgcn_make_buffer_rsrc((void*)wg_, 0, DM * DF * 2, RSRC3);
        const __amdgpu_buffer_rsrc_t ru = __builtin_amdgcn_make_buffer_rsrc((void*)wu_, 0, DM * DF * 2, RSRC3);
        const __amdgpu_buffer_rsrc_t rd = __builtin_amdgcn_make_buffer_rsrc((void*)wd_, 0, DM * DF * 2, RSRC3);
        const int r = lane >> 3, cg = lane & 7;
        const int row0 = 256 * gj + 64 * w;
        const v4u xp = *(const GAS v4u*)((const GAS unsigned char*)x_ + (row0 + 8 * r) * 2);
        const int gv = (8 * r) * (DF * 2) + cg * 16;
        const bool skipA = helper, skipB = slow && !helper;
        const int gvA = skipA ? OOB : gv, gvB = skipB ? OOB : gv;
        const int gso = row0 * (DF * 2) + 128 * gi;
        v4u g0[4], g1[4], u0[4], u1[4];
#pragma unroll
        for (int t = 0; t < 4; ++t) {
            const int v = t < 2 ? gvA : gvB;
            g0[t] = __builtin_amdgcn_raw_buffer_load_b128(rg, v, gso + (2 * t) * (DF * 2), AUX_NT);
            g1[t] = __builtin_amdgcn_raw_buffer_load_b128(rg, v, gso + (2 * t + 1) * (DF * 2), AUX_NT);
            u0[t] = __builtin_amdgcn_raw_buffer_load_b128(ru, v, gso + (2 * t) * (DF * 2), AUX_NT);
            u1[t] = __builtin_amdgcn_raw_buffer_load_b128(ru, v, gso + (2 * t + 1) * (DF * 2), AUX_NT);
        }
        __builtin_amdgcn_sched_barrier(0);
        const int dv = helper ? OOB : (8 * r) * (DM * 2) + cg * 16;
        const int dso = (64 * gi) * (DM * 2) + 512 * gj + 128 * w;
        float a[16];
#pragma unroll
        for (int i = 0; i < 16; ++i) a[i] = 0.f;
#pragma unroll
        for (int t = 0; t < 4; ++t) {
            const unsigned xx = (t < 2 ? skipA : skipB) ? 0u : xp[t];
#pragma unroll
            for (int k = 0; k < 4; ++k) {
                a[2 * k]         = dot2(plo(g0[t][k], g1[t][k]), xx, a[2 * k]);
                a[2 * k + 1]     = dot2(phi(g0[t][k], g1[t][k]), xx, a[2 * k + 1]);
                a[8 + 2 * k]     = dot2(plo(u0[t][k], u1[t][k]), xx, a[8 + 2 * k]);
                a[8 + 2 * k + 1] = dot2(phi(u0[t][k], u1[t][k]), xx, a[8 + 2 * k + 1]);
            }
        }
        asm volatile("" : "+v"(a[0]), "+v"(a[1]), "+v"(a[2]), "+v"(a[3]), "+v"(a[4]), "+v"(a[5]), "+v"(a[6]), "+v"(a[7]));
        asm volatile("" : "+v"(a[8]), "+v"(a[9]), "+v"(a[10]), "+v"(a[11]), "+v"(a[12]), "+v"(a[13]), "+v"(a[14]), "+v"(a[15]));
        __builtin_amdgcn_sched_barrier(0);
        v4u d0[4], d1[4];
#pragma unroll
        for (int t = 0; t < 4; ++t) {
            d0[t] = __builtin_amdgcn_raw_buffer_load_b128(rd, dv, dso + (2 * t) * 2048, AUX_NT);
            d1[t] = __builtin_amdgcn_raw_buffer_load_b128(rd, dv + 2048, dso + (2 * t) * 2048, AUX_NT);
        }
        __builtin_amdgcn_sched_barrier(0);
        {
            float y[8], z[4];
#pragma unroll
            for (int k = 0; k < 8; ++k) y[k] = tr32(a[k], a[k + 8]);
#pragma unroll
            for (int k = 0; k < 4; ++k) z[k] = tr16(y[k], y[k + 4]);
            float2 q; q.x = tr8(z[0], z[2], bit3); q.y = tr8(z[1], z[3], bit3);
            const int col0 = 8 * cg + 4 * ((lane >> 4) & 1) + 2 * ((lane >> 3) & 1);
            *(float2*)&red[(w * 2 + (lane >> 5)) * 64 + col0] = q;
        }
        LBAR();
        if (helper) return;
        unsigned dl[4][4], dh[4][4];
#pragma unroll
        for (int t = 0; t < 4; ++t)
#pragma unroll
            for (int k = 0; k < 4; ++k) { dl[t][k] = plo(d0[t][k], d1[t][k]); dh[t][k] = phi(d0[t][k], d1[t][k]); }
        int qoff = Q_OFF + gi * 8192 + (256 * gj + 64 * w + lane) * 8;
        unsigned T = hs[32];
#pragma unroll
        for (int t = 0; t < 4; ++t) asm volatile("" : "+v"(dl[t][0]), "+v"(dl[t][1]), "+v"(dl[t][2]), "+v"(dl[t][3]), "+v"(dh[t][0]), "+v"(dh[t][1]), "+v"(dh[t][2]), "+v"(dh[t][3]));
        asm volatile("" : "+v"(T), "+v"(qoff));
        LBAR();
        const v4u hp = *(const v4u*)&hs[4 * r];
        float o[8];
#pragma unroll
        for (int i = 0; i < 8; ++i) o[i] = 0.f;
#pragma unroll
        for (int t = 0; t < 4; ++t) {
            const unsigned hh = hp[t];
#pragma unroll
            for (int k = 0; k < 4; ++k) {
                o[2 * k]     = dot2(dl[t][k], hh, o[2 * k]);
                o[2 * k + 1] = dot2(dh[t][k], hh, o[2 * k + 1]);
            }
        }
        float y[4], z[2];
#pragma unroll
        for (int k = 0; k < 4; ++k) y[k] = tr32(o[k], o[k + 4]);
#pragma unroll
        for (int k = 0; k < 2; ++k) z[k] = tr16(y[k], y[k + 2]);
        const float q = tr8(z[0], z[1], bit3);
        const v2u p = {__float_as_uint(q), T};
        __builtin_amdgcn_raw_buffer_store_b64(p, rs, qoff, 0, AUX_SC1);
    }
}

extern "C" void kernel_launch(void* const* d_in, const int* in_sizes, int n_in, void* d_out, int out_size, void* d_ws, size_t ws_size,
                              hipStream_t stream) {
    glu_kernel<<<NWG, 320, 0, stream>>>((const unsigned short*)d_in[0], (const unsigned short*)d_in[1], (const unsigned short*)d_in[2],
                                        (const unsigned short*)d_in[3], (unsigned short*)d_out, (unsigned*)d_ws);
}
```

```cpp
#include <hip/hip_runtime.h>
#include <hip/hip_bf16.h>

#define GAS __attribute__((address_space(1)))
typedef unsigned v4u __attribute__((ext_vector_type(4)));
typedef unsigned v2u __attribute__((ext_vector_type(2)));
typedef __bf16 bf16x2 __attribute__((ext_vector_type(2)));

#define SLEEP1 20

namespace {
constexpr int DM = 1024, DF = 3584;
constexpr int NREG = 224;
constexpr int NWG = 252;
constexpr int P1_OFF = 4096;
constexpr int Q_OFF = P1_OFF + 56 * 8 * 1024;
constexpr int WS_USED = Q_OFF + 56 * 8192;
constexpr int OOB = (int)0x80000000u;
constexpr int AUX_NT = 2, AUX_SC1 = 16;
constexpr int RSRC3 = 0x00020000;
constexpr int CODE_LINES = 34;

__device__ __forceinline__ float dot2(unsigned a, unsigned b, float c) {
    return __builtin_amdgcn_fdot2_f32_bf16(__builtin_bit_cast(bf16x2, a), __builtin_bit_cast(bf16x2, b), c, false);
}
__device__ __forceinline__ unsigned plo(unsigned r0, unsigned r1) { return __builtin_amdgcn_perm(r1, r0, 0x05040100u); }
__device__ __forceinline__ unsigned phi(unsigned r0, unsigned r1) { return __builtin_amdgcn_perm(r1, r0, 0x07060302u); }
__device__ __forceinline__ float bf2f(unsigned b) { return __uint_as_float(b << 16); }
__device__ __forceinline__ unsigned f2bf(float f) { __bf16 h = (__bf16)f; return (unsigned)__builtin_bit_cast(unsigned short, h); }
template <int CTRL> __device__ __forceinline__ float dppf(float v) {
    return __uint_as_float((unsigned)__builtin_amdgcn_update_dpp(0, (int)__float_as_uint(v), CTRL, 0xf, 0xf, true));
}
__device__ __forceinline__ float tr32(float a, float b) { auto r = __builtin_amdgcn_permlane32_swap(__float_as_uint(a), __float_as_uint(b), false, false); return __uint_as_float(r[0]) + __uint_as_float(r[1]); }
__device__ __forceinline__ float tr16(float a, float b) { auto r = __builtin_amdgcn_permlane16_swap(__float_as_uint(a), __float_as_uint(b), false, false); return __uint_as_float(r[0]) + __uint_as_float(r[1]); }
__device__ __forceinline__ float tr8(float a, float b, bool bit3) { const float a2 = a + dppf<0x128>(a), b2 = b + dppf<0x128>(b); return bit3 ? b2 : a2; }
#define GA_STR_(x) #x
#define GA_STR(x) GA_STR_(x)
#define LBAR() do { __builtin_amdgcn_sched_barrier(0); asm volatile("s_waitcnt lgkmcnt(0)\n\ts_barrier" ::: "memory"); __builtin_amdgcn_sched_barrier(0); } while (0)

#define GA_LD(B, B3, O)  "buffer_load_dwordx4 v[" #B ":" #B3 "], %[" #O "], %[rs], 0 offen sc1\n\t"
#define GA_TAKE(W, B, B1, B2, B3, O, R0, R1) \
    "s_waitcnt vmcnt(" #W ")\n\t" \
    "v_cmp_eq_u32 vcc, %[T], v" #B1 "\n\t" \
    "v_cmp_eq_u32 %[m], %[T], v" #B3 "\n\t" \
    "s_and_b64 vcc, vcc, %[m]\n\t" \
    "v_cndmask_b32 v" #R0 ", v" #R0 ", v" #B ", vcc\n\t" \
    "v_cndmask_b32 v" #R1 ", v" #R1 ", v" #B2 ", vcc\n\t" \
    "v_cndmask_b32 %[" #O "], %[" #O "], %[s0], vcc\n\t"
#define GA_END \
    "v_cmp_gt_i32 vcc, 0, %[s1]\n\t" \
    "s_cmp_eq_u64 vcc, exec\n\t" \
    "s_cbranch_scc1 2f\n\t" \
    "s_add_u32 %[c], %[c], 1\n\t" \
    "s_cmp_lt_u32 %[c], 0x10000\n\t" \
    "s_cbranch_scc1 1b\n\t" \
    "2:\n\t"
#define GA_OWN(O, R0, R1) \
    "v_cmp_gt_i32 vcc, 0, %[" #O "]\n\t" \
    "v_mov_b32 v" #R0 ", 0\n\tv_mov_b32 v" #R1 ", 0\n\t" \
    "v_cndmask_b32 v" #R0 ", v" #R0 ", %[i0], vcc\n\t" \
    "v_cndmask_b32 v" #R1 ", v" #R1 ", %[i1], vcc\n\t"
#define GA_ZERO(R0, R1) "v_mov_b32 v" #R0 ", 0\n\tv_mov_b32 v" #R1 ", 0\n\t"

__device__ __forceinline__ void gather_gu(const __amdgpu_buffer_rsrc_t rs, int o0, int o1, int o2, int o3, int o4, int o5, int o6, int o7, float i0, float i1, unsigned T, float& s0, float& s1) {
    unsigned c; unsigned long long m;
    asm volatile(
        GA_OWN(o0, 128, 136) GA_OWN(o1, 129, 137) GA_OWN(o2, 130, 138) GA_OWN(o3, 131, 139)
        GA_ZERO(132, 140) GA_ZERO(133, 141) GA_ZERO(134, 142) GA_ZERO(135, 143)
        "v_bfrev_b32 %[s0], 1\n\t"
        "s_mov_b32 %[c], 0\n\t"
        "s_sleep " GA_STR(SLEEP1) "\n\t"
        "1:\n\t"
        GA_LD(112, 115, o4) GA_LD(116, 119, o5) GA_LD(120, 123, o6) GA_LD(124, 127, o7)
        GA_LD(96, 99, o0) GA_LD(100, 103, o1) GA_LD(104, 107, o2) GA_LD(108, 111, o3)
        GA_TAKE(7, 112, 113, 114, 115, o4, 132, 140)
        GA_TAKE(6, 116, 117, 118, 119, o5, 133, 141)
        GA_TAKE(5, 120, 121, 122, 123, o6, 134, 142)
        GA_TAKE(4, 124, 125, 126, 127, o7, 135, 143)
        "v_and_b32 %[s1], %[o4], %[o5]\n\t"
        "v_and_b32 %[s1], %[s1], %[o6]\n\t"
        "v_and_b32 %[s1], %[s1], %[o7]\n\t"
        GA_TAKE(3, 96, 97, 98, 99, o0, 128, 136)
        GA_TAKE(2, 100, 101, 102, 103, o1, 129, 137)
        GA_TAKE(1, 104, 105, 106, 107, o2, 130, 138)
        GA_TAKE(0, 108, 109, 110, 111, o3, 131, 139)
        "v_and_b32 %[s1], %[s1], %[o0]\n\t"
        "v_and_b32 %[s1], %[s1], %[o1]\n\t"
        "v_and_b32 %[s1], %[s1], %[o2]\n\t"
        "v_and_b32 %[s1], %[s1], %[o3]\n\t"
        GA_END
        "v_add_f32 v128, v128, v129\n\tv_add_f32 v136, v136, v137\n\t"
        "v_add_f32 v130, v130, v131\n\tv_add_f32 v138, v138, v139\n\t"
        "v_add_f32 v132, v132, v133\n\tv_add_f32 v140, v140, v141\n\t"
        "v_add_f32 v134, v134, v135\n\tv_add_f32 v142, v142, v143\n\t"
        "v_add_f32 v128, v128, v130\n\tv_add_f32 v136, v136, v138\n\t"
        "v_add_f32 v132, v132, v134\n\tv_add_f32 v140, v140, v142\n\t"
        "v_add_f32 %[s0], v128, v132\n\tv_add_f32 %[s1], v136, v140\n\t"
        "s_nop 1"
        : [s0] "=&v"(s0), [s1] "=&v"(s1), [c] "=&s"(c), [m] "=&s"(m), [o0] "+v"(o0), [o1] "+v"(o1), [o2] "+v"(o2), [o3] "+v"(o3),
          [o4] "+v"(o4), [o5] "+v"(o5), [o6] "+v"(o6), [o7] "+v"(o7)
        : [i0] "v"(i0), [i1] "v"(i1), [T] "s"(T), [rs] "s"(rs)
        : "v96", "v97", "v98", "v99", "v100", "v101", "v102", "v103", "v104", "v105", "v106", "v107", "v108", "v109", "v110", "v111",
          "v112", "v113", "v114", "v115", "v116", "v117", "v118", "v119", "v120", "v121", "v122", "v123", "v124", "v125", "v126", "v127",
          "v128", "v129", "v130", "v131", "v132", "v133", "v134", "v135", "v136", "v137", "v138", "v139", "v140", "v141", "v142", "v143",
          "vcc", "scc", "memory");
}
__device__ __forceinline__ void gather_out(const __amdgpu_buffer_rsrc_t rs, int o0, int o1, int o2, int o3, unsigned T, float& s0, float& s1) {
    unsigned c; unsigned long long m;
    asm volatile(
        GA_ZERO(128, 136) GA_ZERO(129, 137) GA_ZERO(130, 138) GA_ZERO(131, 139)
        "v_bfrev_b32 %[s0], 1\n\t"
        "s_mov_b32 %[c], 0\n\t"
        "1:\n\t"
        GA_LD(96, 99, o0) GA_LD(100, 103, o1) GA_LD(104, 107, o2) GA_LD(108, 111, o3)
        GA_TAKE(3, 96, 97, 98, 99, o0, 128, 136)
        GA_TAKE(2, 100, 101, 102, 103, o1, 129, 137)
        GA_TAKE(1, 104, 105, 106, 107, o2, 130, 138)
        GA_TAKE(0, 108, 109, 110, 111, o3, 131, 139)
        "v_and_b32 %[s1], %[o0], %[o1]\n\t"
        "v_and_b32 %[s1], %[s1], %[o2]\n\t"
        "v_and_b32 %[s1], %[s1], %[o3]\n\t"
        GA_END
        "v_add_f32 v128, v128, v129\n\tv_add_f32 v136, v136, v137\n\t"
        "v_add_f32 v130, v130, v131\n\tv_add_f32 v138, v138, v139\n\t"
        "v_add_f32 %[s0], v128, v130\n\tv_add_f32 %[s1], v136, v138\n\t"
        "s_nop 1"
        : [s0] "=&v"(s0), [s1] "=&v"(s1), [c] "=&s"(c), [m] "=&s"(m), [o0] "+v"(o0), [o1] "+v"(o1), [o2] "+v"(o2), [o3] "+v"(o3)
        : [T] "s"(T), [rs] "s"(rs)
        : "v96", "v97", "v98", "v99", "v100", "v101", "v102", "v103", "v104", "v105", "v106", "v107", "v108", "v109", "v110", "v111",
          "v128", "v129", "v130", "v131", "v136", "v137", "v138", "v139", "vcc", "scc", "memory");
}
}

__global__ void __launch_bounds__(320, 2)
glu_kernel(const unsigned short* __restrict__ x_, const unsigned short* __restrict__ wg_, const unsigned short* __restrict__ wu_,
           const unsigned short* __restrict__ wd_, unsigned short* __restrict__ out_, unsigned* __restrict__ ws_) {
    __shared__ __attribute__((aligned(16))) float red[4 * 2 * 64];
    __shared__ __attribute__((aligned(16))) unsigned hs[36];
    const int b = blockIdx.x, tid = threadIdx.x, lane = tid & 63;
    const int w = __builtin_amdgcn_readfirstlane(tid >> 6);
    unsigned pfc = 0;
    if (w >= 4) {
        unsigned long long pc; asm volatile("s_getpc_b64 %0" : "=s"(pc));
        pfc = *(const GAS unsigned*)((const GAS unsigned char*)(pc & ~255ull) + min(lane, CODE_LINES - 1) * 128);
    }
    __builtin_amdgcn_sched_barrier(0);
    asm volatile("" :: "s"(x_), "s"(wg_), "s"(wu_), "s"(wd_), "s"(out_), "s"(ws_));
    const __amdgpu_buffer_rsrc_t rs = __builtin_amdgcn_make_buffer_rsrc((void*)ws_, 0, WS_USED, RSRC3);
    const int s = (b & 7) * 28 + (b >> 3);
    const int cls = (3 - (int)((unsigned long long)wg_ >> 7)) & 7;
    const bool helper = b >= NREG;
    const int gi = helper ? cls + 8 * ((b - NREG) >> 2) : s >> 2;
    const int gj = helper ? (b - NREG) & 3 : s & 3;
    const bool slow = (gi & 7) == cls;
    const bool bit3 = (lane & 8) != 0;

    if (w >= 4) {
        const unsigned E = __builtin_amdgcn_raw_buffer_load_b32(rs, 0, 0, AUX_SC1);
        const unsigned pt0 = __builtin_amdgcn_raw_buffer_load_b32(rs, lane < 56 ? Q_OFF + lane * 8192 + (b & 63) * 128 : lane < 58 ? P1_OFF + gi * 8192 + (lane - 56) * 4096 : OOB, 0, AUX_SC1);
        const GAS unsigned char* ptp = (lane & 1) ? (const GAS unsigned char*)out_ + (b & 15) * 128 : (const GAS unsigned char*)wd_ + (size_t)(64 * gi + (lane >> 1) * 2) * (DM * 2) + 512 * gj;
        const unsigned pt1 = __builtin_nontemporal_load((const GAS unsigned*)ptp);
        const int pbase = P1_OFF + gi * 8192 + lane * 16;
        int poff = pbase + (helper ? 4 + gj : gj) * 1024;
        int off[8];
#pragma unroll
        for (int m = 0; m < 8; ++m) off[m] = (m == gj || (m >= 4 && !slow)) ? OOB : pbase + m * 1024;
        const int cidx = (b & 7) * 16 + (b >> 3);
        const int pr = lane & 3, ps = lane >> 2;
        int off2[4];
#pragma unroll
        for (int k = 0; k < 4; ++k) off2[k] = ((k == 3) && (ps >= 8)) ? OOB : Q_OFF + (16 * k + ps) * 8192 + cidx * 64 + pr * 16;
        int ooff = (64 * (cidx >> 3) + 8 * (2 * pr + ((lane >> 4) & 1)) + (cidx & 7)) * 2;
        int rd0 = lane * 4;
        asm volatile("" : "+v"(poff), "+v"(off[0]), "+v"(off[1]), "+v"(off[2]), "+v"(off[3]), "+v"(off[4]), "+v"(off[5]), "+v"(off[6]), "+v"(off[7]));
        asm volatile("" : "+v"(off2[0]), "+v"(off2[1]), "+v"(off2[2]), "+v"(off2[3]), "+v"(ooff), "+v"(rd0));
        unsigned T = __builtin_amdgcn_readfirstlane(max(E + 1u, 1u));
        asm volatile("" : "+s"(T));
        if (lane == 0) hs[32] = T;
        asm volatile("" :: "v"(pfc), "v"(pt0), "v"(pt1));
        LBAR();
        const float* rp = (const float*)((const unsigned char*)red + rd0);
        const float og = (rp[0 * 64] + rp[2 * 64]) + (rp[4 * 64] + rp[6 * 64]);
        const float ou = (rp[1 * 64] + rp[3 * 64]) + (rp[5 * 64] + rp[7 * 64]);
        {
            const v4u p = {__float_as_uint(og), T, __float_as_uint(ou), T};
            __builtin_amdgcn_raw_buffer_store_b128(p, rs, poff, 0, AUX_SC1);
        }
        if (helper) return;
        float g, u;
        gather_gu(rs, off[0], off[1], off[2], off[3], off[4], off[5], off[6], off[7], og, ou, T, g, u);
        {
            const float gb = bf2f(f2bf(g)), ub = bf2f(f2bf(u));
            const float e2 = __builtin_amdgcn_exp2f(gb * __builtin_fmaf(gb * gb, -0.10294324f, -2.3022082f));
            const float ge = gb * __builtin_amdgcn_rcpf(1.0f + e2);
            ((unsigned short*)hs)[lane] = (unsigned short)f2bf(bf2f(f2bf(ge)) * ub);
        }
        LBAR();
        if (b < 128) {
            float s0, s1;
            gather_out(rs, off2[0], off2[1], off2[2], off2[3], T, s0, s1);
            s0 += dppf<0x124>(s0); s1 += dppf<0x124>(s1);
            s0 += dppf<0x128>(s0); s1 += dppf<0x128>(s1);
            const float t16 = tr16(s0, s1);
            const float t32 = tr32(t16, t16);
            if ((lane & 0x2C) == 0) *(GAS unsigned short*)((GAS unsigned char*)out_ + ooff) = (unsigned short)f2bf(t32);
            if (b == 0 && lane == 0) *(GAS unsigned*)ws_ = T;
        }
    } else {
        const __amdgpu_buffer_rsrc_t rg = __builtin_amdgcn_make_buffer_rsrc((void*)wg_, 0, DM * DF * 2, RSRC3);
        const __amdgpu_buffer_rsrc_t ru = __builtin_amdgcn_make_buffer_rsrc((void*)wu_, 0, DM * DF * 2, RSRC3);
        const __amdgpu_buffer_rsrc_t rd = __builtin_amdgcn_make_buffer_rsrc((void*)wd_, 0, DM * DF * 2, RSRC3);
        const int r = lane >> 3, cg = lane & 7;
        const int row0 = 256 * gj + 64 * w;
        const v4u xp = *(const GAS v4u*)((const GAS unsigned char*)x_ + (row0 + 8 * r) * 2);
        const int gv = (8 * r) * (DF * 2) + cg * 16;
        const bool skipA = helper, skipB = slow && !helper;
        const int gvA = skipA ? OOB : gv, gvB = skipB ? OOB : gv;
        const int gso = row0 * (DF * 2) + 128 * gi;
        v4u g0[4], g1[4], u0[4], u1[4];
#pragma unroll
        for (int t = 0; t < 4; ++t) {
            const int v = t < 2 ? gvA : gvB;
            g0[t] = __builtin_amdgcn_raw_buffer_load_b128(rg, v, gso + (2 * t) * (DF * 2), AUX_NT);
            g1[t] = __builtin_amdgcn_raw_buffer_load_b128(rg, v, gso + (2 * t + 1) * (DF * 2), AUX_NT);
            u0[t] = __builtin_amdgcn_raw_buffer_load_b128(ru, v, gso + (2 * t) * (DF * 2), AUX_NT);
            u1[t] = __builtin_amdgcn_raw_buffer_load_b128(ru, v, gso + (2 * t + 1) * (DF * 2), AUX_NT);
        }
        __builtin_amdgcn_sched_barrier(0);
        const int dv = helper ? OOB : (8 * r) * (DM * 2) + cg * 16;
        const int dso = (64 * gi) * (DM * 2) + 512 * gj + 128 * w;
        float a[16];
#pragma unroll
        for (int i = 0; i < 16; ++i) a[i] = 0.f;
#pragma unroll
        for (int t = 0; t < 4; ++t) {
            const unsigned xx = (t < 2 ? skipA : skipB) ? 0u : xp[t];
#pragma unroll
            for (int k = 0; k < 4; ++k) {
                a[2 * k]         = dot2(plo(g0[t][k], g1[t][k]), xx, a[2 * k]);
                a[2 * k + 1]     = dot2(phi(g0[t][k], g1[t][k]), xx, a[2 * k + 1]);
                a[8 + 2 * k]     = dot2(plo(u0[t][k], u1[t][k]), xx, a[8 + 2 * k]);
                a[8 + 2 * k + 1] = dot2(phi(u0[t][k], u1[t][k]), xx, a[8 + 2 * k + 1]);
            }
        }
        asm volatile("" : "+v"(a[0]), "+v"(a[1]), "+v"(a[2]), "+v"(a[3]), "+v"(a[4]), "+v"(a[5]), "+v"(a[6]), "+v"(a[7]));
        asm volatile("" : "+v"(a[8]), "+v"(a[9]), "+v"(a[10]), "+v"(a[11]), "+v"(a[12]), "+v"(a[13]), "+v"(a[14]), "+v"(a[15]));
        __builtin_amdgcn_sched_barrier(0);
        v4u d0[4], d1[4];
#pragma unroll
        for (int t = 0; t < 4; ++t) {
            d0[t] = __builtin_amdgcn_raw_buffer_load_b128(rd, dv, dso + (2 * t) * 2048, AUX_NT);
            d1[t] = __builtin_amdgcn_raw_buffer_load_b128(rd, dv + 2048, dso + (2 * t) * 2048, AUX_NT);
        }
        __builtin_amdgcn_sched_barrier(0);
        {
            float y[8], z[4];
#pragma unroll
            for (int k = 0; k < 8; ++k) y[k] = tr32(a[k], a[k + 8]);
#pragma unroll
            for (int k = 0; k < 4; ++k) z[k] = tr16(y[k], y[k + 4]);
            float2 q; q.x = tr8(z[0], z[2], bit3); q.y = tr8(z[1], z[3], bit3);
            const int col0 = 8 * cg + 4 * ((lane >> 4) & 1) + 2 * ((lane >> 3) & 1);
            *(float2*)&red[(w * 2 + (lane >> 5)) * 64 + col0] = q;
        }
        LBAR();
        if (helper) return;
        unsigned dl[4][4], dh[4][4];
#pragma unroll
        for (int t = 0; t < 4; ++t)
#pragma unroll
            for (int k = 0; k < 4; ++k) { dl[t][k] = plo(d0[t][k], d1[t][k]); dh[t][k] = phi(d0[t][k], d1[t][k]); }
        int qoff = Q_OFF + gi * 8192 + (256 * gj + 64 * w + lane) * 8;
        unsigned T = hs[32];
#pragma unroll
        for (int t = 0; t < 4; ++t) asm volatile("" : "+v"(dl[t][0]), "+v"(dl[t][1]), "+v"(dl[t][2]), "+v"(dl[t][3]), "+v"(dh[t][0]), "+v"(dh[t][1]), "+v"(dh[t][2]), "+v"(dh[t][3]));
        asm volatile("" : "+v"(T), "+v"(qoff));
        LBAR();
        const v4u hp = *(const v4u*)&hs[4 * r];
        float o[8];
#pragma unroll
        for (int i = 0; i < 8; ++i) o[i] = 0.f;
#pragma unroll
        for (int t = 0; t < 4; ++t) {
            const unsigned hh = hp[t];
#pragma unroll
            for (int k = 0; k < 4; ++k) {
                o[2 * k]     = dot2(dl[t][k], hh, o[2 * k]);
                o[2 * k + 1] = dot2(dh[t][k], hh, o[2 * k + 1]);
            }
        }
        float y[4], z[2];
#pragma unroll
        for (int k = 0; k < 4; ++k) y[k] = tr32(o[k], o[k + 4]);
#pragma unroll
        for (int k = 0; k < 2; ++k) z[k] = tr16(y[k], y[k + 2]);
        const float q = tr8(z[0], z[1], bit3);
        const v2u p = {__float_as_uint(q), T};
        __builtin_amdgcn_raw_buffer_store_b64(p, rs, qoff, 0, AUX_SC1);
    }
}

extern "C" void kernel_launch(void* const* d_in, const int* in_sizes, int n_in, void* d_out, int out_size, void* d_ws, size_t ws_size,
                              hipStream_t stream) {
    glu_kernel<<<NWG, 320, 0, stream>>>((const unsigned short*)d_in[0], (const unsigned short*)d_in[1], (const unsigned short*)d_in[2],
                                        (const unsigned short*)d_in[3], (unsigned short*)d_out, (unsigned*)d_ws);
}
```

```cpp
#include <hip/hip_runtime.h>

#define GAS __attribute__((address_space(1)))
typedef unsigned v4u __attribute__((ext_vector_type(4)));
typedef unsigned v2u __attribute__((ext_vector_type(2)));
typedef __bf16 bf16x2 __attribute__((ext_vector_type(2)));

#define SLEEP1 20

namespace {
constexpr int DM = 1024, DF = 3584;
constexpr int NREG = 224;
constexpr int NWG = 252;
constexpr int P1_OFF = 4096;
constexpr int Q_OFF = P1_OFF + 56 * 8 * 1024;
constexpr int WS_USED = Q_OFF + 56 * 8192;
constexpr int OOB = (int)0x80000000u;
constexpr int AUX_NT = 2, AUX_SC1 = 16;
constexpr int RSRC3 = 0x00020000;
constexpr int CODE_LINES = 32;

__device__ __forceinline__ float dot2(unsigned a, unsigned b, float c) {
    return __builtin_amdgcn_fdot2_f32_bf16(__builtin_bit_cast(bf16x2, a), __builtin_bit_cast(bf16x2, b), c, false);
}
__device__ __forceinline__ unsigned plo(unsigned r0, unsigned r1) { return __builtin_amdgcn_perm(r1, r0, 0x05040100u); }
__device__ __forceinline__ unsigned phi(unsigned r0, unsigned r1) { return __builtin_amdgcn_perm(r1, r0, 0x07060302u); }
__device__ __forceinline__ float bf2f(unsigned b) { return __uint_as_float(b << 16); }
__device__ __forceinline__ unsigned f2bf(float f) { __bf16 h = (__bf16)f; return (unsigned)__builtin_bit_cast(unsigned short, h); }
template <int CTRL> __device__ __forceinline__ float dppf(float v) {
    return __uint_as_float((unsigned)__builtin_amdgcn_update_dpp(0, (int)__float_as_uint(v), CTRL, 0xf, 0xf, true));
}
__device__ __forceinline__ float tr32(float a, float b) { auto r = __builtin_amdgcn_permlane32_swap(__float_as_uint(a), __float_as_uint(b), false, false); return __uint_as_float(r[0]) + __uint_as_float(r[1]); }
__device__ __forceinline__ float tr16(float a, float b) { auto r = __builtin_amdgcn_permlane16_swap(__float_as_uint(a), __float_as_uint(b), false, false); return __uint_as_float(r[0]) + __uint_as_float(r[1]); }
__device__ __forceinline__ float tr8(float a, float b, bool bit3) { const float a2 = a + dppf<0x128>(a), b2 = b + dppf<0x128>(b); return bit3 ? b2 : a2; }
#define GA_STR_(x) #x
#define GA_STR(x) GA_STR_(x)
#define LBAR() do { __builtin_amdgcn_sched_barrier(0); asm volatile("s_waitcnt lgkmcnt(0)\n\ts_barrier" ::: "memory"); __builtin_amdgcn_sched_barrier(0); } while (0)

#define GA_LD(B, B3, O)  "buffer_load_dwordx4 v[" #B ":" #B3 "], %[" #O "], %[rs], 0 offen sc1\n\t"
#define GA_TAKE(W, B, B1, B2, B3, O, R0, R1) \
    "s_waitcnt vmcnt(" #W ")\n\t" \
    "v_cmp_eq_u32 vcc, %[T], v" #B1 "\n\t" \
    "v_cmp_eq_u32 %[m], %[T], v" #B3 "\n\t" \
    "s_and_b64 vcc, vcc, %[m]\n\t" \
    "v_cndmask_b32 v" #R0 ", v" #R0 ", v" #B ", vcc\n\t" \
    "v_cndmask_b32 v" #R1 ", v" #R1 ", v" #B2 ", vcc\n\t" \
    "v_cndmask_b32 %[" #O "], %[" #O "], %[s0], vcc\n\t"
#define GA_END \
    "v_cmp_gt_i32 vcc, 0, %[s1]\n\t" \
    "s_cmp_eq_u64 vcc, exec\n\t" \
    "s_cbranch_scc1 2f\n\t" \
    "s_add_u32 %[c], %[c], 1\n\t" \
    "s_cmp_lt_u32 %[c], 0x10000\n\t" \
    "s_cbranch_scc1 1b\n\t" \
    "2:\n\t"
#define GA_OWN(O, R0, R1) \
    "v_cmp_gt_i32 vcc, 0, %[" #O "]\n\t" \
    "v_mov_b32 v" #R0 ", 0\n\tv_mov_b32 v" #R1 ", 0\n\t" \
    "v_cndmask_b32 v" #R0 ", v" #R0 ", %[i0], vcc\n\t" \
    "v_cndmask_b32 v" #R1 ", v" #R1 ", %[i1], vcc\n\t"
#define GA_ZERO(R0, R1) "v_mov_b32 v" #R0 ", 0\n\tv_mov_b32 v" #R1 ", 0\n\t"

__device__ __forceinline__ void gather_gu(const __amdgpu_buffer_rsrc_t rs, int o0, int o1, int o2, int o3, int o4, int o5, int o6, int o7, float i0, float i1, unsigned T, float& s0, float& s1) {
    unsigned c; unsigned long long m;
    asm volatile(
        GA_OWN(o0, 128, 136) GA_OWN(o1, 129, 137) GA_OWN(o2, 130, 138) GA_OWN(o3, 131, 139)
        GA_ZERO(132, 140) GA_ZERO(133, 141) GA_ZERO(134, 142) GA_ZERO(135, 143)
        "v_bfrev_b32 %[s0], 1\n\t"
        "s_mov_b32 %[c], 0\n\t"
        "s_sleep " GA_STR(SLEEP1) "\n\t"
        "1:\n\t"
        GA_LD(112, 115, o4) GA_LD(116, 119, o5) GA_LD(120, 123, o6) GA_LD(124, 127, o7)
        GA_LD(96, 99, o0) GA_LD(100, 103, o1) GA_LD(104, 107, o2) GA_LD(108, 111, o3)
        GA_TAKE(7, 112, 113, 114, 115, o4, 132, 140)
        GA_TAKE(6, 116, 117, 118, 119, o5, 133, 141)
        GA_TAKE(5, 120, 121, 122, 123, o6, 134, 142)
        GA_TAKE(4, 124, 125, 126, 127, o7, 135, 143)
        "v_and_b32 %[s1], %[o4], %[o5]\n\t"
        "v_and_b32 %[s1], %[s1], %[o6]\n\t"
        "v_and_b32 %[s1], %[s1], %[o7]\n\t"
        GA_TAKE(3, 96, 97, 98, 99, o0, 128, 136)
        GA_TAKE(2, 100, 101, 102, 103, o1, 129, 137)
        GA_TAKE(1, 104, 105, 106, 107, o2, 130, 138)
        GA_TAKE(0, 108, 109, 110, 111, o3, 131, 139)
        "v_and_b32 %[s1], %[s1], %[o0]\n\t"
        "v_and_b32 %[s1], %[s1], %[o1]\n\t"
        "v_and_b32 %[s1], %[s1], %[o2]\n\t"
        "v_and_b32 %[s1], %[s1], %[o3]\n\t"
        GA_END
        "v_add_f32 v128, v128, v129\n\tv_add_f32 v136, v136, v137\n\t"
        "v_add_f32 v130, v130, v131\n\tv_add_f32 v138, v138, v139\n\t"
        "v_add_f32 v132, v132, v133\n\tv_add_f32 v140, v140, v141\n\t"
        "v_add_f32 v134, v134, v135\n\tv_add_f32 v142, v142, v143\n\t"
        "v_add_f32 v128, v128, v130\n\tv_add_f32 v136, v136, v138\n\t"
        "v_add_f32 v132, v132, v134\n\tv_add_f32 v140, v140, v142\n\t"
        "v_add_f32 %[s0], v128, v132\n\tv_add_f32 %[s1], v136, v140\n\t"
        "s_nop 1"
        : [s0] "=&v"(s0), [s1] "=&v"(s1), [c] "=&s"(c), [m] "=&s"(m), [o0] "+v"(o0), [o1] "+v"(o1), [o2] "+v"(o2), [o3] "+v"(o3),
          [o4] "+v"(o4), [o5] "+v"(o5), [o6] "+v"(o6), [o7] "+v"(o7)
        : [i0] "v"(i0), [i1] "v"(i1), [T] "s"(T), [rs] "s"(rs)
        : "v96", "v97", "v98", "v99", "v100", "v101", "v102", "v103", "v104", "v105", "v106", "v107", "v108", "v109", "v110", "v111",
          "v112", "v113", "v114", "v115", "v116", "v117", "v118", "v119", "v120", "v121", "v122", "v123", "v124", "v125", "v126", "v127",
          "v128", "v129", "v130", "v131", "v132", "v133", "v134", "v135", "v136", "v137", "v138", "v139", "v140", "v141", "v142", "v143",
          "vcc", "scc", "memory");
}
__device__ __forceinline__ void gather_out(const __amdgpu_buffer_rsrc_t rs, int o0, int o1, int o2, int o3, unsigned T, float& s0, float& s1) {
    unsigned c; unsigned long long m;
    asm volatile(
        GA_ZERO(128, 136) GA_ZERO(129, 137) GA_ZERO(130, 138) GA_ZERO(131, 139)
        "v_bfrev_b32 %[s0], 1\n\t"
        "s_mov_b32 %[c], 0\n\t"
        "1:\n\t"
        GA_LD(96, 99, o0) GA_LD(100, 103, o1) GA_LD(104, 107, o2) GA_LD(108, 111, o3)
        GA_TAKE(3, 96, 97, 98, 99, o0, 128, 136)
        GA_TAKE(2, 100, 101, 102, 103, o1, 129, 137)
        GA_TAKE(1, 104, 105, 106, 107, o2, 130, 138)
        GA_TAKE(0, 108, 109, 110, 111, o3, 131, 139)
        "v_and_b32 %[s1], %[o0], %[o1]\n\t"
        "v_and_b32 %[s1], %[s1], %[o2]\n\t"
        "v_and_b32 %[s1], %[s1], %[o3]\n\t"
        GA_END
        "v_add_f32 v128, v128, v129\n\tv_add_f32 v136, v136, v137\n\t"
        "v_add_f32 v130, v130, v131\n\tv_add_f32 v138, v138, v139\n\t"
        "v_add_f32 %[s0], v128, v130\n\tv_add_f32 %[s1], v136, v138\n\t"
        "s_nop 1"
        : [s0] "=&v"(s0), [s1] "=&v"(s1), [c] "=&s"(c), [m] "=&s"(m), [o0] "+v"(o0), [o1] "+v"(o1), [o2] "+v"(o2), [o3] "+v"(o3)
        : [T] "s"(T), [rs] "s"(rs)
        : "v96", "v97", "v98", "v99", "v100", "v101", "v102", "v103", "v104", "v105", "v106", "v107", "v108", "v109", "v110", "v111",
          "v128", "v129", "v130", "v131", "v136", "v137", "v138", "v139", "vcc", "scc", "memory");
}
}

__global__ void __launch_bounds__(320, 2)
glu_kernel(const unsigned short* __restrict__ x_, const unsigned short* __restrict__ wg_, const unsigned short* __restrict__ wu_,
           const unsigned short* __restrict__ wd_, unsigned short* __restrict__ out_, unsigned* __restrict__ ws_) {
    __shared__ __attribute__((aligned(16))) float red[4 * 2 * 64];
    __shared__ __attribute__((aligned(16))) unsigned hs[36 + 21504];
    const int b = blockIdx.x, tid = threadIdx.x, lane = tid & 63;
    const int w = __builtin_amdgcn_readfirstlane(tid >> 6);
    unsigned pfc = 0;
    if (w >= 4) {
        unsigned long long pc; asm volatile("s_getpc_b64 %0" : "=s"(pc));
        pfc = *(const GAS unsigned*)((const GAS unsigned char*)(pc & ~255ull) + min(lane, CODE_LINES - 1) * 128);
    }
    __builtin_amdgcn_sched_barrier(0);
    asm volatile("" :: "s"(x_), "s"(wg_), "s"(wu_), "s"(wd_), "s"(out_), "s"(ws_));
    const __amdgpu_buffer_rsrc_t rs = __builtin_amdgcn_make_buffer_rsrc((void*)ws_, 0, WS_USED, RSRC3);
    const int s = (b & 7) * 28 + (b >> 3);
    const int cls = (3 - (int)((unsigned long long)wg_ >> 7)) & 7;
    const bool helper = b >= NREG;
    const int gi = helper ? cls + 8 * ((b - NREG) >> 2) : s >> 2;
    const int gj = helper ? (b - NREG) & 3 : s & 3;
    const bool slow = (gi & 7) == cls;
    const bool bit3 = (lane & 8) != 0;

    if (w >= 4) {
        const unsigned E = __builtin_amdgcn_raw_buffer_load_b32(rs, 0, 0, AUX_SC1);
        const int pbase = P1_OFF + gi * 8192 + lane * 16;
        int poff = pbase + (helper ? 4 + gj : gj) * 1024;
        int off[8];
#pragma unroll
        for (int m = 0; m < 8; ++m) off[m] = (m == gj || (m >= 4 && !slow)) ? OOB : pbase + m * 1024;
        const int cidx = (b & 7) * 16 + (b >> 3);
        const int pr = lane & 3, ps = lane >> 2;
        int off2[4];
#pragma unroll
        for (int k = 0; k < 4; ++k) off2[k] = ((k == 3) && (ps >= 8)) ? OOB : Q_OFF + (16 * k + ps) * 8192 + cidx * 64 + pr * 16;
        int ooff = (64 * (cidx >> 3) + 8 * (2 * pr + ((lane >> 4) & 1)) + (cidx & 7)) * 2;
        int rd0 = lane * 4;
        asm volatile("" : "+v"(poff), "+v"(off[0]), "+v"(off[1]), "+v"(off[2]), "+v"(off[3]), "+v"(off[4]), "+v"(off[5]), "+v"(off[6]), "+v"(off[7]));
        asm volatile("" : "+v"(off2[0]), "+v"(off2[1]), "+v"(off2[2]), "+v"(off2[3]), "+v"(ooff), "+v"(rd0));
        unsigned T = __builtin_amdgcn_readfirstlane(max(E + 1u, 1u));
        asm volatile("" : "+s"(T));
        if (lane == 0) hs[32] = T;
        asm volatile("" :: "v"(pfc));
        LBAR();
        const float* rp = (const float*)((const unsigned char*)red + rd0);
        const float og = (rp[0 * 64] + rp[2 * 64]) + (rp[4 * 64] + rp[6 * 64]);
        const float ou = (rp[1 * 64] + rp[3 * 64]) + (rp[5 * 64] + rp[7 * 64]);
        {
            const v4u p = {__float_as_uint(og), T, __float_as_uint(ou), T};
            __builtin_amdgcn_raw_buffer_store_b128(p, rs, poff, 0, AUX_SC1);
        }
        if (helper) return;
        float g, u;
        gather_gu(rs, off[0], off[1], off[2], off[3], off[4], off[5], off[6], off[7], og, ou, T, g, u);
        {
            const float gb = bf2f(f2bf(g)), ub = bf2f(f2bf(u));
            const float e2 = __builtin_amdgcn_exp2f(gb * __builtin_fmaf(gb * gb, -0.10294324f, -2.3022082f));
            const float ge = gb * __builtin_amdgcn_rcpf(1.0f + e2);
            ((unsigned short*)hs)[lane] = (unsigned short)f2bf(bf2f(f2bf(ge)) * ub);
        }
        LBAR();
        if (b < 128) {
            float s0, s1;
            gather_out(rs, off2[0], off2[1], off2[2], off2[3], T, s0, s1);
            s0 += dppf<0x124>(s0); s1 += dppf<0x124>(s1);
            s0 += dppf<0x128>(s0); s1 += dppf<0x128>(s1);
            const float t16 = tr16(s0, s1);
            const float t32 = tr32(t16, t16);
            if ((lane & 0x2C) == 0) *(GAS unsigned short*)((GAS unsigned char*)out_ + ooff) = (unsigned short)f2bf(t32);
            if (b == 0 && lane == 0) *(GAS unsigned*)ws_ = T;
        }
    } else {
        const __amdgpu_buffer_rsrc_t rg = __builtin_amdgcn_make_buffer_rsrc((void*)wg_, 0, DM * DF * 2, RSRC3);
        const __amdgpu_buffer_rsrc_t ru = __builtin_amdgcn_make_buffer_rsrc((void*)wu_, 0, DM * DF * 2, RSRC3);
        const __amdgpu_buffer_rsrc_t rd = __builtin_amdgcn_make_buffer_rsrc((void*)wd_, 0, DM * DF * 2, RSRC3);
        const int r = lane >> 3, cg = lane & 7;
        const int row0 = 256 * gj + 64 * w;
        const v4u xp = *(const GAS v4u*)((const GAS unsigned char*)x_ + (row0 + 8 * r) * 2);
        const int gv = (8 * r) * (DF * 2) + cg * 16;
        const bool skipA = helper, skipB = slow && !helper;
        const int gvA = skipA ? OOB : gv, gvB = skipB ? OOB : gv;
        const int gso = row0 * (DF * 2) + 128 * gi;
        v4u g0[4], g1[4], u0[4], u1[4];
#pragma unroll
        for (int t = 0; t < 4; ++t) {
            const int v = t < 2 ? gvA : gvB;
            g0[t] = __builtin_amdgcn_raw_buffer_load_b128(rg, v, gso + (2 * t) * (DF * 2), AUX_NT);
            g1[t] = __builtin_amdgcn_raw_buffer_load_b128(rg, v, gso + (2 * t + 1) * (DF * 2), AUX_NT);
            u0[t] = __builtin_amdgcn_raw_buffer_load_b128(ru, v, gso + (2 * t) * (DF * 2), AUX_NT);
            u1[t] = __builtin_amdgcn_raw_buffer_load_b128(ru, v, gso + (2 * t + 1) * (DF * 2), AUX_NT);
        }
        __builtin_amdgcn_sched_barrier(0);
        const int dv = helper ? OOB : (8 * r) * (DM * 2) + cg * 16;
        const int dso = (64 * gi) * (DM * 2) + 512 * gj + 128 * w;
        float a[16];
#pragma unroll
        for (int i = 0; i < 16; ++i) a[i] = 0.f;
#pragma unroll
        for (int t = 0; t < 4; ++t) {
            const unsigned xx = (t < 2 ? skipA : skipB) ? 0u : xp[t];
#pragma unroll
            for (int k = 0; k < 4; ++k) {
                a[2 * k]         = dot2(plo(g0[t][k], g1[t][k]), xx, a[2 * k]);
                a[2 * k + 1]     = dot2(phi(g0[t][k], g1[t][k]), xx, a[2 * k + 1]);
                a[8 + 2 * k]     = dot2(plo(u0[t][k], u1[t][k]), xx, a[8 + 2 * k]);
                a[8 + 2 * k + 1] = dot2(phi(u0[t][k], u1[t][k]), xx, a[8 + 2 * k + 1]);
            }
        }
        asm volatile("" : "+v"(a[0]), "+v"(a[1]), "+v"(a[2]), "+v"(a[3]), "+v"(a[4]), "+v"(a[5]), "+v"(a[6]), "+v"(a[7]));
        asm volatile("" : "+v"(a[8]), "+v"(a[9]), "+v"(a[10]), "+v"(a[11]), "+v"(a[12]), "+v"(a[13]), "+v"(a[14]), "+v"(a[15]));
        __builtin_amdgcn_sched_barrier(0);
        v4u d0[4], d1[4];
#pragma unroll
        for (int t = 0; t < 4; ++t) {
            d0[t] = __builtin_amdgcn_raw_buffer_load_b128(rd, dv, dso + (2 * t) * 2048, AUX_NT);
            d1[t] = __builtin_amdgcn_raw_buffer_load_b128(rd, dv + 2048, dso + (2 * t) * 2048, AUX_NT);
        }
        __builtin_amdgcn_sched_barrier(0);
        {
            float y[8], z[4];
#pragma unroll
            for (int k = 0; k < 8; ++k) y[k] = tr32(a[k], a[k + 8]);
#pragma unroll
            for (int k = 0; k < 4; ++k) z[k] = tr16(y[k], y[k + 4]);
            float2 q; q.x = tr8(z[0], z[2], bit3); q.y = tr8(z[1], z[3], bit3);
            const int col0 = 8 * cg + 4 * ((lane >> 4) & 1) + 2 * ((lane >> 3) & 1);
            *(float2*)&red[(w * 2 + (lane >> 5)) * 64 + col0] = q;
        }
        LBAR();
        if (helper) return;
        unsigned dl[4][4], dh[4][4];
#pragma unroll
        for (int t = 0; t < 4; ++t)
#pragma unroll
            for (int k = 0; k < 4; ++k) { dl[t][k] = plo(d0[t][k], d1[t][k]); dh[t][k] = phi(d0[t][k], d1[t][k]); }
        int qoff = Q_OFF + gi * 8192 + (256 * gj + 64 * w + lane) * 8;
        unsigned T = hs[32];
#pragma unroll
        for (int t = 0; t < 4; ++t) asm volatile("" : "+v"(dl[t][0]), "+v"(dl[t][1]), "+v"(dl[t][2]), "+v"(dl[t][3]), "+v"(dh[t][0]), "+v"(dh[t][1]), "+v"(dh[t][2]), "+v"(dh[t][3]));
        asm volatile("" : "+v"(T), "+v"(qoff));
        LBAR();
        const v4u hp = *(const v4u*)&hs[4 * r];
        float o[8];
#pragma unroll
        for (int i = 0; i < 8; ++i) o[i] = 0.f;
#pragma unroll
        for (int t = 0; t < 4; ++t) {
            const unsigned hh = hp[t];
#pragma unroll
            for (int k = 0; k < 4; ++k) {
                o[2 * k]     = dot2(dl[t][k], hh, o[2 * k]);
                o[2 * k + 1] = dot2(dh[t][k], hh, o[2 * k + 1]);
            }
        }
        float y[4], z[2];
#pragma unroll
        for (int k = 0; k < 4; ++k) y[k] = tr32(o[k], o[k + 4]);
#pragma unroll
        for (int k = 0; k < 2; ++k) z[k] = tr16(y[k], y[k + 2]);
        const float q = tr8(z[0], z[1], bit3);
        const v2u p = {__float_as_uint(q), T};
        __builtin_amdgcn_raw_buffer_store_b64(p, rs, qoff, 0, AUX_SC1);
    }
}

extern "C" void kernel_launch(void* const* d_in, const int* in_sizes, int n_in, void* d_out, int out_size, void* d_ws, size_t ws_size,
                              hipStream_t stream) {
    glu_kernel<<<NWG, 320, 0, stream>>>((const unsigned short*)d_in[0], (const unsigned short*)d_in[1], (const unsigned short*)d_in[2],
                                        (const unsigned short*)d_in[3], (unsigned short*)d_out, (unsigned*)d_ws);
}
```
